```python
import torch

def _softmax(x, axis=-1):
    return torch.softmax(x, dim=axis)
def _gelu(x):
    return torch.nn.functional.gelu(x, approximate="tanh")
def _silu(x):
    return torch.nn.functional.silu(x)
def _sigmoid(x):
    return torch.sigmoid(x)
def _layernorm(x, axis=-1, eps=1e-5):
    m = x.mean(dim=axis, keepdim=True)
    v = x.var(dim=axis, keepdim=True, unbiased=False)
    return (x - m) / torch.sqrt(v + eps)
def _scatter_add(tgt, idx, src, axis=0):
    return tgt.clone().index_add_(axis, idx, src)
def _segment_sum(src, seg, n):
    out = torch.zeros((n,) + src.shape[1:], dtype=src.dtype)
    return out.index_add_(0, seg, src)
def _pad(x, pw):
    flat = tuple(v for pair in reversed(pw) for v in pair)
    return torch.nn.functional.pad(x, flat)

kernel_name = 'sequential_preLN__mha_block__dense_activation_squared_relu__prompt'

def setup_inputs():

    _w = (lambda a: (lambda t: t.to(torch.bfloat16) if t.is_floating_point() else t)(torch.as_tensor(a)))
    def _A_setup():
        B, S, D, H = 1, 2048, 640, 32
        return {
            "x": _w((lambda *s: torch.randn(*s, dtype=torch.bfloat16) * 0.05)(B, S, D)),
            "wqkv": _w((lambda *s: torch.randn(*s, dtype=torch.bfloat16) * 0.05)(D, 3 * D)),
            "wo": _w((lambda *s: torch.randn(*s, dtype=torch.bfloat16) * 0.05)(D, D)),
        }
    _A_in = _A_setup()

    def _B_setup():
        B, S, D, F = 1, 2048, 640, 1728
        return {
            "x": _w((lambda *s: torch.randn(*s, dtype=torch.bfloat16) * 0.05)(B, S, D)),
            "w1": _w((lambda *s: torch.randn(*s, dtype=torch.bfloat16) * 0.05)(D, F)),
            "b1": _w((lambda *s: torch.randn(*s, dtype=torch.bfloat16) * 0.4)(F)),
            "w2": _w((lambda *s: torch.randn(*s, dtype=torch.bfloat16) * 0.05)(F, D)),
            "b2": _w((lambda *s: torch.randn(*s, dtype=torch.bfloat16) * 3.2)(D)),
        }
    _B_in = _B_setup()

    return {
        "A_x": _A_in["x"],
        "A_wqkv": _A_in["wqkv"],
        "A_wo": _A_in["wo"],
        "B_x": _B_in["x"],
        "B_w1": _B_in["w1"],
        "B_b1": _B_in["b1"],
        "B_w2": _B_in["w2"],
        "B_b2": _B_in["b2"],
    }

def reference(A_x, A_wqkv, A_wo, B_x, B_w1, B_b1, B_w2, B_b2):
    def _A_ref(x, wqkv, wo):
        B, S, D = x.shape
        H = 32
        Dh = D // H
        qkv = (lambda x, *d: x.permute(*d))((x @ wqkv).reshape(B, S, 3, H, Dh), 2, 0, 3, 1, 4)
        q, k, v = qkv[0], qkv[1], qkv[2]
        scores = torch.einsum("bhqd,bhkd->bhqk", q, k) * (Dh ** -0.5)
        p = _softmax(scores, axis=-1)
        o = torch.einsum("bhqk,bhkd->bhqd", p, v)
        return (lambda x, *d: x.permute(*d))(o, 0, 2, 1, 3).reshape(B, S, D) @ wo

    def _B_ref(x, w1, b1, w2, b2):
        h = torch.einsum("bsd,df->bsf", x, w1) + b1
        zero = torch.zeros_like(h)
        r = torch.where(h > zero, h, zero)
        h = r * r
        out = torch.einsum("bsf,fd->bsd", h, w2) + b2
        return out

    x = A_x
    h = x + _A_ref(_layernorm(x, axis=-1), A_wqkv, A_wo)
    h2 = h + _B_ref(_layernorm(h, axis=-1), B_w1, B_b1, B_w2, B_b2)
    return h2
```

```cpp
#include <hip/hip_runtime.h>
#include <hip/hip_bf16.h>
#include <stdint.h>

typedef __bf16 bf2 __attribute__((ext_vector_type(2)));
typedef __bf16 bf8 __attribute__((ext_vector_type(8)));
typedef short s16x4 __attribute__((ext_vector_type(4)));
typedef short s16x8 __attribute__((ext_vector_type(8)));
typedef float f32x2 __attribute__((ext_vector_type(2)));
typedef float f32x4 __attribute__((ext_vector_type(4)));
typedef float f32x16 __attribute__((ext_vector_type(16)));
typedef unsigned u32x2 __attribute__((ext_vector_type(2)));
typedef unsigned u32x4 __attribute__((ext_vector_type(4)));
#define LAS __attribute__((address_space(3)))
#define DEV __device__ __forceinline__
#define SBAR() __builtin_amdgcn_sched_barrier(0)

constexpr int S_ = 2048, D_ = 640, H_ = 32, F_ = 1728;
constexpr size_t WS_Q = 0;
constexpr size_t WS_KV = WS_Q + 32 * 64 * 1280;
constexpr size_t WS_KN = WS_KV + 32 * 64 * 2560;
constexpr size_t WS_O = WS_KN + 4096;
constexpr size_t WS_WO = WS_O + 128 * 80 * 256;
constexpr size_t WS_W2 = WS_WO + 40 * 20 * 1024;
constexpr size_t WS_H = WS_W2 + 40 * 54 * 1024;
constexpr size_t WS_ST = WS_H + 2048 * 640 * 2;
constexpr size_t WS_HID = WS_ST + 2048 * 8 * 8;
constexpr size_t WS_END = WS_HID + 128 * 216 * 256;

constexpr float QSCALE = 0.22360679774997896f * 1.4426950408889634f;
constexpr float LN_EPS = 1e-5f;

DEV unsigned cvtpk(float lo, float hi) { f32x2 v = {lo, hi}; return __builtin_bit_cast(unsigned, __builtin_convertvector(v, bf2)); }
DEV float bflo(unsigned u) { return __uint_as_float(u << 16); }
DEV float bfhi(unsigned u) { return __uint_as_float(u & 0xffff0000u); }
DEV float dot2(unsigned a, unsigned b, float c) { return __builtin_amdgcn_fdot2_f32_bf16(__builtin_bit_cast(bf2, a), __builtin_bit_cast(bf2, b), c, false); }
DEV f32x16 mfma32(u32x4 a, u32x4 b, f32x16 c) { return __builtin_amdgcn_mfma_f32_32x32x16_bf16(__builtin_bit_cast(bf8, a), __builtin_bit_cast(bf8, b), c, 0, 0, 0); }
DEV f32x16 mfma32k8(u32x2 a, u32x2 b, f32x16 c) { return __builtin_amdgcn_mfma_f32_32x32x8bf16_1k(__builtin_bit_cast(s16x4, a), __builtin_bit_cast(s16x4, b), c, 0, 0, 0); }
DEV f32x4 mfma16(u32x4 a, u32x4 b, f32x4 c) { return __builtin_amdgcn_mfma_f32_16x16x32_bf16(__builtin_bit_cast(bf8, a), __builtin_bit_cast(bf8, b), c, 0, 0, 0); }
DEV float swap32_add(float v) { auto r = __builtin_amdgcn_permlane32_swap(__float_as_uint(v), __float_as_uint(v), false, false); return __uint_as_float(r[0]) + __uint_as_float(r[1]); }
DEV float swap32_max(float v) { auto r = __builtin_amdgcn_permlane32_swap(__float_as_uint(v), __float_as_uint(v), false, false); return fmaxf(__uint_as_float(r[0]), __uint_as_float(r[1])); }
DEV float other32(float v, int hi) { auto r = __builtin_amdgcn_permlane32_swap(__float_as_uint(v), __float_as_uint(v), false, false); return __uint_as_float(hi ? r[0] : r[1]); }
DEV void glds16(unsigned voff, const void* sbase, unsigned ldsaddr) {
    unsigned sv;
    asm volatile("s_mov_b32 %0, m0\n\ts_mov_b32 m0, %3\n\ts_nop 0\n\tglobal_load_lds_dwordx4 %1, %2\n\ts_mov_b32 m0, %0"
                 : "=&s"(sv) : "v"(voff), "s"(sbase), "s"(ldsaddr) : "memory");
}
#define WAITV(n) asm volatile("s_waitcnt vmcnt(" #n ")" ::: "memory")
#define WAITL0() asm volatile("s_waitcnt lgkmcnt(0)" ::: "memory")
#define BAR() do { asm volatile("" ::: "memory"); __builtin_amdgcn_s_barrier(); asm volatile("" ::: "memory"); } while (0)
DEV u32x4 lds128(LAS const char* p) { return *(LAS const u32x4*)p; }
DEV u32x2 lds64(LAS const char* p) { return *(LAS const u32x2*)p; }
DEV u32x2 ldstr(LAS const char* p) { return __builtin_bit_cast(u32x2, __builtin_amdgcn_ds_read_tr16_b64_v4i16((LAS s16x4*)p)); }

constexpr int G_STAGE = 32768, G_RING = 4 * G_STAGE;
constexpr int G_STAT = G_RING;
constexpr int G_CS = G_STAT + 1024;
constexpr int G_NORM = G_CS + 512;
constexpr int G_LDS = G_NORM + 4096;

template <int WROW  >
DEV void g_loader(const char* xb, const char* wb, int l, int lane, int wcol  , unsigned lds0) {
    const int r8 = lane >> 3, pos = lane & 7;
    const unsigned xo0 = (unsigned)(r8 * 1280 + ((pos ^ ((lane >> 4) & 7)) * 16));
    const unsigned xo1 = (unsigned)(r8 * 1280 + ((pos ^ (4 + (lane >> 4))) * 16));
    const int r4 = lane >> 4, p16 = lane & 15;
    int cbyte = wcol + ((p16 ^ (r4 << 2)) * 16);
    if (cbyte >= WROW) cbyte -= 128;
    const unsigned wo = (unsigned)(r4 * WROW + cbyte);
    const char* xs = xb + (size_t)l * 32 * 1280;
    const char* ws = wb + (size_t)l * 16 * WROW;
    auto issue = [&](int s) {
        const unsigned dst = lds0 + (unsigned)(s & 3) * G_STAGE + (unsigned)l * 4096;
        const char* xp = xs + s * 128;
        const char* wp = ws + (size_t)s * 64 * WROW;
#pragma unroll
        for (int i = 0; i < 4; ++i) glds16((i & 1) ? xo1 : xo0, xp + i * 8 * 1280, dst + i * 1024);
#pragma unroll
        for (int i = 0; i < 4; ++i) glds16(wo, wp + (size_t)i * 4 * WROW, dst + 16384 + i * 1024);
    };
    issue(0); issue(1); issue(2); issue(3);
    WAITV(24); BAR();
#pragma unroll 1
    for (int s = 1; s < 7; ++s) { WAITV(16); BAR(); issue(s + 3); }
    WAITV(16); BAR();
    WAITV(8); BAR();
    WAITV(0); BAR();
}

template <bool VORI, bool XSTATS>
DEV void g_consumer(LAS const char* lds, int ct, int cc, int lane, f32x16 (&acc)[2][2], float& sx, float& sxx, float& cs) {
    const int r = lane & 31, hh = lane >> 5;
    unsigned xa[2][4], wa[2];
    const int sw = (r >> 1) & 7;
#pragma unroll
    for (int ks = 0; ks < 4; ++ks) {
        xa[0][ks] = (unsigned)((2 * ct + cc) * 4096 + r * 128 + (((2 * ks) ^ hh ^ sw) * 16));
        xa[1][ks] = xa[0][ks] ^ 4096u;
    }
    {
        const int gam = lane >> 4, q = (lane & 15) >> 2, pp = lane & 3;
#pragma unroll
        for (int v = 0; v < 2; ++v) {
            const int T = 2 * cc + (v ^ ct);
            wa[v] = (unsigned)(16384 + (8 * (gam >> 1) + q) * 256 + (((T ^ q) * 4 + 2 * (gam & 1) + (pp >> 1)) * 16) + (pp & 1) * 8);
        }
    }
#pragma unroll
    for (int u = 0; u < 2; ++u)
#pragma unroll
        for (int v = 0; v < 2; ++v) acc[u][v] = f32x16{};
    sx = 0.f; sxx = 0.f; cs = 0.f;
    const unsigned ONES = 0x3f803f80u;
    u32x4 xf[2][2]; u32x2 wl[2][2], wh[2][2];
#define RD(set, boff, ks) do { \
        xf[set][0] = lds128(lds + (boff) + xa[0][ks]); xf[set][1] = lds128(lds + (boff) + xa[1][ks]); \
        wl[set][0] = ldstr(lds + (boff) + wa[0] + (ks) * 4096); wh[set][0] = ldstr(lds + (boff) + wa[0] + (ks) * 4096 + 1024); \
        wl[set][1] = ldstr(lds + (boff) + wa[1] + (ks) * 4096); wh[set][1] = ldstr(lds + (boff) + wa[1] + (ks) * 4096 + 1024); } while (0)
#define MM(set) do { \
        const u32x4 w0 = {wl[set][0][0], wl[set][0][1], wh[set][0][0], wh[set][0][1]}; \
        const u32x4 w1 = {wl[set][1][0], wl[set][1][1], wh[set][1][0], wh[set][1][1]}; \
        if (VORI) { acc[0][0] = mfma32(xf[set][0], w0, acc[0][0]); acc[0][1] = mfma32(xf[set][0], w1, acc[0][1]); \
                    acc[1][0] = mfma32(xf[set][1], w0, acc[1][0]); acc[1][1] = mfma32(xf[set][1], w1, acc[1][1]); } \
        else      { acc[0][0] = mfma32(w0, xf[set][0], acc[0][0]); acc[0][1] = mfma32(w1, xf[set][0], acc[0][1]); \
                    acc[1][0] = mfma32(w0, xf[set][1], acc[1][0]); acc[1][1] = mfma32(w1, xf[set][1], acc[1][1]); } \
        _Pragma("unroll") for (int j = 0; j < 4; ++j) { cs = dot2(w0[j], ONES, cs); \
            if (XSTATS) { sx = dot2(xf[set][0][j], ONES, sx); sxx = dot2(xf[set][0][j], xf[set][0][j], sxx); } } } while (0)
    BAR();
    RD(0, 0, 0);
    unsigned boff = 0;
#pragma unroll 1
    for (int s = 0; s < 10; ++s) {
        RD(1, boff, 1); MM(0);
        RD(0, boff, 2); MM(1);
        RD(1, boff, 3); MM(0);
        if (s < 9) {
            WAITL0(); BAR();
            boff = (boff + G_STAGE) & (G_RING - 1);
            RD(0, boff, 0);
        }
        MM(1);
    }
#undef RD
#undef MM
}

template <int MODE>
__global__ __launch_bounds__(512, 2) void k_gemm128(const char* __restrict__ xin, const char* __restrict__ win,
                                                    const char* __restrict__ bias, char* __restrict__ ws) {
    extern __shared__ __attribute__((aligned(16))) char smem[];
    const int b = blockIdx.x, xcd = b & 7, i = b >> 3;
    int rb, cb;
    if (MODE == 0) { rb = (xcd & 1) * 8 + (i & 7); cb = (xcd >> 1) + 4 * (i >> 3); if (cb >= 15) return; }
    else           { rb = (xcd & 1) * 8 + (i & 7); cb = (xcd >> 1) + 4 * (i >> 3); if (cb >= 14) return; }
    const int tid = threadIdx.x, lane = tid & 63, wid = __builtin_amdgcn_readfirstlane(tid >> 6);
    const unsigned lds0 = (unsigned)(uintptr_t)smem;
    LAS char* lds = (LAS char*)smem;
    constexpr int WROW = MODE == 0 ? 3840 : 3456;
    const char* xb = (MODE == 0 ? xin : (const char*)(ws + WS_H)) + (size_t)rb * 128 * 1280;
    if (wid >= 4) { g_loader<WROW>(xb, win, wid - 4, lane, cb * 256, lds0); return; }

    const int ct = wid & 1, cc = wid >> 1, r = lane & 31, hi = lane >> 5;
    LAS f32x2* stat = (LAS f32x2*)(lds + G_STAT);
    LAS float* csum = (LAS float*)(lds + G_CS);
    LAS float* norm = (LAS float*)(lds + G_NORM);
    if (MODE == 1) {
        if (tid < 128) {
            const f32x4* p = (const f32x4*)(ws + WS_ST + (size_t)(rb * 128 + tid) * 64);
            const f32x4 a = p[0], bb = p[1], c = p[2], d = p[3];
            const float s1 = (a[0] + a[2]) + (bb[0] + bb[2]) + (c[0] + c[2]) + (d[0] + d[2]);
            const float s2 = (a[1] + a[3]) + (bb[1] + bb[3]) + (c[1] + c[3]) + (d[1] + d[3]);
            const float mu = s1 * (1.f / 640.f), var = fmaxf(s2 * (1.f / 640.f) - mu * mu, 0.f);
            stat[tid] = (f32x2){mu, 1.f / sqrtf(var + LN_EPS)};
        }
    } else {
        for (int k = tid; k < 1024; k += 256) norm[k] = 0.f;
    }
    f32x16 acc[2][2]; float sx, sxx, cs;
    const bool vori = (MODE == 0) && cb >= 10;
    if (vori) g_consumer<true, MODE == 0>(lds, ct, cc, lane, acc, sx, sxx, cs);
    else g_consumer<false, MODE == 0>(lds, ct, cc, lane, acc, sx, sxx, cs);

    cs = swap32_add(cs);
    if (hi == 0) csum[(2 * cc + ct) * 32 + r] = cs;
    if (MODE == 0) {
        sx = swap32_add(sx); sxx = swap32_add(sxx);
        const float mu = sx * (1.f / 640.f), var = fmaxf(sxx * (1.f / 640.f) - mu * mu, 0.f);
        if (hi == 0) stat[(2 * ct + cc) * 32 + r] = (f32x2){mu, 1.f / sqrtf(var + LN_EPS)};
    }
    WAITL0(); BAR();

    if (MODE == 1) {
        const __hip_bfloat16* b1 = (const __hip_bfloat16*)bias;
#pragma unroll
        for (int v = 0; v < 2; ++v) {
            const int T = 2 * cc + (v ^ ct), cb32 = cb * 4 + T;
            if (cb32 >= 54) continue;
            f32x4 c4[4], b4[4];
#pragma unroll
            for (int a = 0; a < 4; ++a) {
                c4[a] = *(LAS const f32x4*)(csum + T * 32 + 8 * a + 4 * hi);
                const u32x2 bb = *(const u32x2*)((const char*)b1 + (cb32 * 32 + 8 * a + 4 * hi) * 2);
                b4[a] = (f32x4){bflo(bb[0]), bfhi(bb[0]), bflo(bb[1]), bfhi(bb[1])};
            }
#pragma unroll
            for (int u = 0; u < 2; ++u) {
                const int tt = 2 * ct + (u ^ cc);
                const f32x2 st = stat[tt * 32 + r];
                float h[16];
#pragma unroll
                for (int k = 0; k < 16; ++k) {
                    const float z = fmaf(fmaf(-st[0], c4[k >> 2][k & 3], acc[u][v][k]), st[1], b4[k >> 2][k & 3]);
                    const float rl = fmaxf(z, 0.f);
                    h[k] = rl * rl;
                }
                const int tok = rb * 128 + tt * 32 + r;
#pragma unroll
                for (int s = 0; s < 2; ++s) {
                    const u32x4 w = {cvtpk(h[8 * s], h[8 * s + 1]), cvtpk(h[8 * s + 2], h[8 * s + 3]), cvtpk(h[8 * s + 4], h[8 * s + 5]), cvtpk(h[8 * s + 6], h[8 * s + 7])};
                    const int q = (cb32 * 2 + s) * 2 + hi;
                    *(u32x4*)(ws + WS_HID + ((size_t)((tok >> 4) * 216 + q) * 16 + (tok & 15)) * 16) = w;
                }
            }
        }
        return;
    }
    if (vori) {
#pragma unroll
        for (int u = 0; u < 2; ++u) {
            const int tt = 2 * ct + (u ^ cc);
            f32x2 st[16];
#pragma unroll
            for (int a = 0; a < 4; ++a) {
                const f32x4 s0 = *(LAS const f32x4*)(stat + tt * 32 + 8 * a + 4 * hi), s1 = *(LAS const f32x4*)(stat + tt * 32 + 8 * a + 4 * hi + 2);
                st[4 * a] = (f32x2){s0[0], s0[1]}; st[4 * a + 1] = (f32x2){s0[2], s0[3]};
                st[4 * a + 2] = (f32x2){s1[0], s1[1]}; st[4 * a + 3] = (f32x2){s1[2], s1[3]};
            }
#pragma unroll
            for (int v = 0; v < 2; ++v) {
                const int T = 2 * cc + (v ^ ct);
                const int col = (cb - 10) * 128 + T * 32 + r, head = col / 20, d = col - head * 20;
                const float c = csum[T * 32 + r];
                float h[16];
#pragma unroll
                for (int k = 0; k < 16; ++k) h[k] = fmaf(-st[k][0], c, acc[u][v][k]) * st[k][1];
                char* rec = ws + WS_KV + ((size_t)head * 64 + rb * 4 + tt) * 2560 + 1280 + hi * 320 + d * 16;
#pragma unroll
                for (int s = 0; s < 2; ++s) {
                    const u32x4 w = {cvtpk(h[8 * s], h[8 * s + 1]), cvtpk(h[8 * s + 2], h[8 * s + 3]), cvtpk(h[8 * s + 4], h[8 * s + 5]), cvtpk(h[8 * s + 6], h[8 * s + 7])};
                    *(u32x4*)(rec + s * 640) = w;
                }
            }
        }
        return;
    }
    const bool isK = cb >= 5;
    const int cbl = isK ? cb - 5 : cb;
    const int head0 = (32 * cbl) / 5;
#pragma unroll
    for (int v = 0; v < 2; ++v) {
        const int T = 2 * cc + (v ^ ct);
        f32x4 c4[4];
#pragma unroll
        for (int a = 0; a < 4; ++a) c4[a] = *(LAS const f32x4*)(csum + T * 32 + 8 * a + 4 * hi);
#pragma unroll
        for (int u = 0; u < 2; ++u) {
            const int tt = 2 * ct + (u ^ cc);
            const f32x2 st = stat[tt * 32 + r];
            const float sc = isK ? st[1] : st[1] * QSCALE;
#pragma unroll
            for (int a = 0; a < 4; ++a) {
                float h[4];
#pragma unroll
                for (int k = 0; k < 4; ++k) h[k] = fmaf(-st[0], c4[a][k], acc[u][v][4 * a + k]) * sc;
                const int gg = 32 * cbl + T * 8 + 2 * a + hi, head = gg / 5, g = gg - head * 5;
                const u32x2 w = {cvtpk(h[0], h[1]), cvtpk(h[2], h[3])};
                char* rec = isK ? ws + WS_KV + ((size_t)head * 64 + rb * 4 + tt) * 2560 : ws + WS_Q + ((size_t)head * 64 + rb * 4 + tt) * 1280;
                const int off = g < 4 ? (g >> 1) * 512 + r * 16 + (g & 1) * 8 : 1024 + r * 8;
                *(u32x2*)(rec + off) = w;
                if (isK) {
                    const float n2 = (h[0] * h[0] + h[1] * h[1]) + (h[2] * h[2] + h[3] * h[3]);
                    __hip_atomic_fetch_add(norm + (head - head0) * 128 + tt * 32 + r, n2, __ATOMIC_RELAXED, __HIP_MEMORY_SCOPE_WORKGROUP);
                }
            }
        }
    }
    if (isK) {
        WAITL0(); BAR();
#pragma unroll
        for (int hl = 0; hl < 2; ++hl) {
            const int lh = wid * 2 + hl;
            float m = fmaxf(norm[lh * 128 + lane], norm[lh * 128 + 64 + lane]);
#pragma unroll
            for (int o = 32; o >= 1; o >>= 1) m = fmaxf(m, __shfl_xor(m, o));
            if (lane == 0) ((float*)(ws + WS_KN))[(rb * 5 + cbl) * 8 + lh] = m;
        }
    }
}

constexpr int A_STAGE = 40960, A_RING = 3 * A_STAGE;
constexpr int A_ONES = A_RING;
constexpr int A_FLAG = A_ONES + 8 * 2560;
constexpr int A_LDS = A_FLAG + 64;

DEV void prep_addr(int task, int lane, const char* wo, const char* w2, const char*& base, unsigned& o0, unsigned& o1, unsigned& dst) {
    const int col = lane & 15, g = lane >> 4;
    if (task < 800) {
        const int ct = task % 40, ks = task / 40;
        const int q = 4 * ks + g, p = q / 5, c = q - 5 * p;
        int k0, k1;
        if (c < 4) { k0 = (2 * p + (c >> 1)) * 20 + 4 * (c & 1); k1 = k0 + 8; }
        else { k0 = (2 * p) * 20 + 16; k1 = k0 + 20; }
        base = wo; o0 = (unsigned)(k0 * 1280 + (ct * 16 + col) * 2); o1 = (unsigned)(k1 * 1280 + (ct * 16 + col) * 2);
        dst = (unsigned)(WS_WO + (size_t)(ct * 20 + ks) * 1024 + lane * 16);
    } else {
        const int t = task - 800, ct = t % 40, ks = t / 40;
        const int k0 = 32 * ks + 16 * (g >> 1) + 4 * (g & 1);
        base = w2; o0 = (unsigned)(k0 * 1280 + (ct * 16 + col) * 2); o1 = o0 + 8 * 1280;
        dst = (unsigned)(WS_W2 + (size_t)(ct * 54 + ks) * 1024 + lane * 16);
    }
}
DEV void prep_task(int task, int lane, const char* wo, const char* w2, char* ws) {
    const char* base; unsigned o0, o1, dst;
    prep_addr(task, lane, wo, w2, base, o0, o1, dst);
    unsigned short e[8];
#pragma unroll
    for (int j = 0; j < 4; ++j) { e[j] = *(const unsigned short*)(base + o0 + j * 1280); e[4 + j] = *(const unsigned short*)(base + o1 + j * 1280); }
    const u32x4 w = {e[0] | ((unsigned)e[1] << 16), e[2] | ((unsigned)e[3] << 16), e[4] | ((unsigned)e[5] << 16), e[6] | ((unsigned)e[7] << 16)};
    *(u32x4*)(ws + dst) = w;
}

__global__ __launch_bounds__(512, 2) void k_attn(const char* __restrict__ wo, const char* __restrict__ w2, char* __restrict__ ws) {
    extern __shared__ __attribute__((aligned(16))) char smem[];
    const int b = blockIdx.x, xcd = b & 7, i = b >> 3;
    const int p = 2 * xcd + (i >> 4), rb = i & 15;
    const int tid = threadIdx.x, lane = tid & 63, wid = __builtin_amdgcn_readfirstlane(tid >> 6);
    const int hp = wid >> 2, head = 2 * p + hp, qt = rb * 4 + (wid & 3);
    const int r = lane & 31, hi = lane >> 5;
    const unsigned lds0 = (unsigned)(uintptr_t)smem;
    LAS char* lds = (LAS char*)smem;

    u32x4 q1; u32x2 q2; float knv;
    const int kb0 = (5 * head) / 32, kb1 = (5 * head + 4) / 32;
    {
        const char* qb = ws + WS_Q + ((size_t)head * 64 + qt) * 1280;
        const unsigned o1 = lane * 16, o2 = 1024 + r * 8;
        const int kb = (lane & 16) ? kb1 : kb0;
        const unsigned o3 = (unsigned)((((lane & 15) * 5 + kb) * 8 + head - (32 * kb) / 5) * 4);
        const char* knb = ws + WS_KN;
        asm volatile("global_load_dwordx4 %0, %3, %6\n\tglobal_load_dwordx2 %1, %4, %6\n\tglobal_load_dword %2, %5, %7"
                     : "=&v"(q1), "=&v"(q2), "=&v"(knv) : "v"(o1), "v"(o2), "v"(o3), "s"(qb), "s"(knb) : "memory");
    }
    const char* kvb = ws + WS_KV + (size_t)head * 64 * 2560 + (wid & 3) * 5120;
    const unsigned dsto = lds0 + hp * 20480 + (wid & 3) * 5120;
    const unsigned lo16 = lane * 16;
    auto issue = [&](int s, int buf) {
#pragma unroll
        for (int k = 0; k < 5; ++k) glds16(lo16, kvb + (size_t)s * 20480 + k * 1024, dsto + buf * A_STAGE + k * 1024);
    };
    issue(0, 0); issue(1, 1); issue(2, 2);
    const int gw = b * 8 + wid;
    if (tid < 16) *(LAS u32x4*)(lds + A_ONES + (tid >> 1) * 2560 + (tid & 1) * 640 + 128) = (u32x4){0x3f803f80u, 0x3f803f80u, 0x3f803f80u, 0x3f803f80u};
    if (tid == 0) *(LAS unsigned*)(lds + A_FLAG) = 0;
    asm volatile("s_waitcnt vmcnt(10)" : "+v"(q1), "+v"(q2), "+v"(knv) : : "memory");
    if ((lane & 16) && kb1 == kb0) knv = 0.f;
#pragma unroll
    for (int o_ = 1; o_ < 16; o_ <<= 1) knv = fmaxf(knv, __shfl_xor(knv, o_));
    const float kn2 = knv + __shfl_xor(knv, 16);
    if (hi) q2 = (u32x2){0, 0};
    float qn2 = 0.f;
#pragma unroll
    for (int k = 0; k < 4; ++k) qn2 = dot2(q1[k], q1[k], qn2);
    qn2 = dot2(q2[0], q2[0], qn2); qn2 = dot2(q2[1], q2[1], qn2);
    qn2 = swap32_add(qn2);
    const float mq = sqrtf(qn2 * kn2) * 1.02f + 1e-3f;
    const bool unsafe = !(mq <= 50.f);
    WAITL0(); BAR();
    if (__any(unsafe) && lane == 0) *(LAS unsigned*)(lds + A_FLAG) = 1;
    WAITL0(); BAR();
    const bool slow = __builtin_amdgcn_readfirstlane(*(LAS const unsigned*)(lds + A_FLAG)) != 0;

    const unsigned ka = hp * 20480 + lane * 16;
    const unsigned k2a = hp * 20480 + 1024 + r * 8;
    unsigned va = hp * 20480 + 1280 + hi * 320 + r * 16;
    const bool vdata = r < 20 || r >= 28;
    if (r >= 28) va = hp * 20480 + 1280 + hi * 320 + 16 * 16;
    if (!vdata) va = A_ONES + 128;
    f32x16 o = f32x16{};

    if (!slow) {
        f32x16 negm;
#pragma unroll
        for (int k = 0; k < 16; ++k) negm[k] = -mq;
        unsigned kp = ka, k2p = k2a, vp = va;
        u32x4 k1f, v0f, v1f, p0, p1; u32x2 k2f;
        f32x16 sa, sb;
        k1f = lds128(lds + kp); k2f = lds64(lds + k2p);
        sa = mfma32(k1f, q1, negm); sa = mfma32k8(k2f, q2, sa);
        k1f = lds128(lds + kp + 2560); k2f = lds64(lds + k2p + 2560);
        v0f = lds128(lds + vp);
        v1f = (u32x4){0, 0, 0, 0}; p1 = (u32x4){0, 0, 0, 0};
#define EXP4(S, k) do { S[k] = __builtin_amdgcn_exp2f(S[k]); S[k + 1] = __builtin_amdgcn_exp2f(S[k + 1]); S[k + 2] = __builtin_amdgcn_exp2f(S[k + 2]); S[k + 3] = __builtin_amdgcn_exp2f(S[k + 3]); } while (0)
#define PIN(x) asm volatile("" : "+v"(x))
#define TILE(S, N, KP, K2P, KO, VP1, VO1, VP0, VO0) do { \
        N = mfma32(k1f, q1, negm); k1f = lds128(lds + (KP) + (KO)); \
        EXP4(S, 0); p0[0] = cvtpk(S[0], S[1]); p0[1] = cvtpk(S[2], S[3]); PIN(p0); SBAR(); \
        o = mfma32(v1f, p1, o); v1f = lds128(lds + (VP1) + (VO1) + 640); \
        EXP4(S, 4); p0[2] = cvtpk(S[4], S[5]); p0[3] = cvtpk(S[6], S[7]); PIN(p0); SBAR(); \
        N = mfma32k8(k2f, q2, N); k2f = lds64(lds + (K2P) + (KO)); \
        EXP4(S, 8); p1[0] = cvtpk(S[8], S[9]); p1[1] = cvtpk(S[10], S[11]); PIN(p1); SBAR(); \
        o = mfma32(v0f, p0, o); v0f = lds128(lds + (VP0) + (VO0)); \
        EXP4(S, 12); p1[2] = cvtpk(S[12], S[13]); p1[3] = cvtpk(S[14], S[15]); PIN(p1); SBAR(); } while (0)
        int buf = 0;
#pragma unroll 1
        for (int s = 0; s < 8; ++s) {
            TILE(sa, sb, kp, k2p, 2 * 2560, vp, 0 * 2560, vp, 1 * 2560);
            TILE(sb, sa, kp, k2p, 3 * 2560, vp, 1 * 2560, vp, 2 * 2560);
            TILE(sa, sb, kp, k2p, 4 * 2560, vp, 2 * 2560, vp, 3 * 2560);
            TILE(sb, sa, kp, k2p, 5 * 2560, vp, 3 * 2560, vp, 4 * 2560);
            TILE(sa, sb, kp, k2p, 6 * 2560, vp, 4 * 2560, vp, 5 * 2560);
            TILE(sb, sa, kp, k2p, 7 * 2560, vp, 5 * 2560, vp, 6 * 2560);
            const int nb = buf == 2 ? 0 : buf + 1;
            const unsigned kn = ka + nb * A_STAGE, k2n = k2a + nb * A_STAGE, vn = va + (vdata ? nb * A_STAGE : 0);
            if (s < 7) {
                WAITV(0); WAITL0(); BAR();
                if (s >= 1 && s < 6) issue(s + 2, buf == 0 ? 2 : buf - 1);
                if (s == 1 + 2 * hp) {
                    prep_task(gw, lane, wo, w2, ws);
                    if (gw + 2048 < 2960) prep_task(gw + 2048, lane, wo, w2, ws);
                }
            }
            TILE(sa, sb, kn, k2n, 0, vp, 6 * 2560, vp, 7 * 2560);
            TILE(sb, sa, kn, k2n, 2560, vp, 7 * 2560, vn, 0);
            kp = kn; k2p = k2n; vp = vn; buf = nb;
        }
        o = mfma32(v1f, p1, o);
    } else {
        float m = -INFINITY;
        int buf = 0;
#pragma unroll 1
        for (int s = 0; s < 8; ++s) {
            if (s > 0) {
                WAITV(0); WAITL0(); BAR();
                if (s >= 1 && s < 6) issue(s + 2, buf == 0 ? 2 : buf - 1);
                if (s == 1) {
                    prep_task(gw, lane, wo, w2, ws);
                    if (gw + 2048 < 2960) prep_task(gw + 2048, lane, wo, w2, ws);
                }
            }
            const unsigned so = buf * A_STAGE, vo = vdata ? so : 0;
#pragma unroll 1
            for (int t = 0; t < 8; ++t) {
                const u32x4 k1f = lds128(lds + ka + so + t * 2560); const u32x2 k2f = lds64(lds + k2a + so + t * 2560);
                const u32x4 v0f = lds128(lds + va + vo + t * 2560), v1f = lds128(lds + va + vo + t * 2560 + 640);
                f32x16 sc = mfma32(k1f, q1, f32x16{}); sc = mfma32k8(k2f, q2, sc);
                float tm = sc[0];
#pragma unroll
                for (int k = 1; k < 16; ++k) tm = fmaxf(tm, sc[k]);
                tm = swap32_max(tm);
                const float mn = fmaxf(m, tm), al = __builtin_amdgcn_exp2f(m - mn);
                m = mn;
#pragma unroll
                for (int k = 0; k < 16; ++k) { o[k] *= al; sc[k] = __builtin_amdgcn_exp2f(sc[k] - mn); }
                const u32x4 p0 = {cvtpk(sc[0], sc[1]), cvtpk(sc[2], sc[3]), cvtpk(sc[4], sc[5]), cvtpk(sc[6], sc[7])};
                const u32x4 p1 = {cvtpk(sc[8], sc[9]), cvtpk(sc[10], sc[11]), cvtpk(sc[12], sc[13]), cvtpk(sc[14], sc[15])};
                o = mfma32(v0f, p0, o); o = mfma32(v1f, p1, o);
            }
            buf = buf == 2 ? 0 : buf + 1;
        }
    }
    const float lo_ = other32(o[8], hi);
    const float l = hi ? o[8] : lo_;
    const float il = 1.f / l;
    const int tok = qt * 32 + r;
    char* ob = ws + WS_O + ((size_t)((tok >> 4) * 80 + 5 * p) * 16 + (tok & 15)) * 16;
    const u32x4 w = {cvtpk(o[0] * il, o[1] * il), cvtpk(o[2] * il, o[3] * il), cvtpk(o[4] * il, o[5] * il), cvtpk(o[6] * il, o[7] * il)};
    *(u32x4*)(ob + (2 * hp + hi) * 256) = w;
    if (hi == 0) *(u32x2*)(ob + 4 * 256 + hp * 8) = (u32x2){cvtpk(o[8] * il, o[9] * il), cvtpk(o[10] * il, o[11] * il)};
}

constexpr int R_LDS = 61440;
template <int MODE>
__global__ __launch_bounds__(256, 1) void k_gemm64(const char* __restrict__ xin, const char* __restrict__ bias, char* __restrict__ ws, char* __restrict__ out) {
    extern __shared__ __attribute__((aligned(16))) char smem[];
    constexpr int NK = MODE == 0 ? 20 : 54, NCH = 4 * NK, DEPTH = MODE == 0 ? 5 : 6, NMAX = (NK + 3) / 4;
    const int b = blockIdx.x, xcd = b & 7, i = b >> 3;
    const int rb = (xcd >> 1) * 8 + (i & 7), cb = (xcd & 1) * 4 + (i >> 3);
    const int tid = threadIdx.x, lane = tid & 63, w = __builtin_amdgcn_readfirstlane(tid >> 6);
    const char* act = ws + (MODE == 0 ? WS_O : WS_HID) + (size_t)lane * 16;
    const char* wf = ws + (MODE == 0 ? WS_WO : WS_W2) + (size_t)cb * 5 * NK * 1024 + (size_t)lane * 16;
    const char* ap[4];
#pragma unroll
    for (int u = 0; u < 4; ++u) ap[u] = act + (size_t)(rb * 4 + ((u + w) & 3)) * NCH * 256;
    f32x4 acc[4][5];
#pragma unroll
    for (int u = 0; u < 4; ++u)
#pragma unroll
        for (int v = 0; v < 5; ++v) acc[u][v] = f32x4{};
    u32x4 af[DEPTH][4], bf[DEPTH][5];
    const int nmine = (NK - w + 3) / 4;
#define LOADSTEP(slot, n) do { const int ks_ = w + 4 * (n); \
        _Pragma("unroll") for (int v = 0; v < 5; ++v) bf[slot][v] = *(const u32x4*)(wf + ((size_t)v * NK + ks_) * 1024); \
        _Pragma("unroll") for (int u = 0; u < 4; ++u) af[slot][u] = *(const u32x4*)(ap[u] + (size_t)ks_ * 1024); } while (0)
#pragma unroll
    for (int n = 0; n < DEPTH; ++n) LOADSTEP(n, n);
    SBAR();
#pragma unroll
    for (int n = 0; n < NMAX; ++n) {
        if (n < NK / 4 || n < nmine) {
#pragma unroll
            for (int u = 0; u < 4; ++u)
#pragma unroll
                for (int v = 0; v < 5; ++v) acc[u][v] = mfma16(bf[n % DEPTH][v], af[n % DEPTH][u], acc[u][v]);
            SBAR();
            if (n + DEPTH < NMAX && (n + DEPTH < NK / 4 || n + DEPTH < nmine)) LOADSTEP(n % DEPTH, n + DEPTH);
            SBAR();
        }
    }
#undef LOADSTEP
    LAS char* lds = (LAS char*)smem;
#pragma unroll
    for (int u = 1; u < 4; ++u)
#pragma unroll
        for (int v = 0; v < 5; ++v) *(LAS f32x4*)(lds + ((((u + w) & 3) * 3 + (u - 1)) * 5 + v) * 1024 + lane * 16) = acc[u][v];
    const int tk = lane & 15, g = lane >> 4, tok = rb * 64 + w * 16 + tk;
    u32x2 res[5], bb[5];
#pragma unroll
    for (int v = 0; v < 5; ++v) {
        const int col = cb * 80 + v * 16 + 4 * g;
        res[v] = *(const u32x2*)((MODE == 0 ? xin : (const char*)(ws + WS_H)) + (size_t)tok * 1280 + col * 2);
        if (MODE == 1) bb[v] = *(const u32x2*)(bias + col * 2);
    }
    WAITL0(); BAR();
    float s1 = 0.f, s2 = 0.f;
#pragma unroll
    for (int v = 0; v < 5; ++v) {
        f32x4 a = acc[0][v];
#pragma unroll
        for (int k = 0; k < 3; ++k) a += *(LAS const f32x4*)(lds + ((w * 3 + k) * 5 + v) * 1024 + lane * 16);
        a += (f32x4){bflo(res[v][0]), bfhi(res[v][0]), bflo(res[v][1]), bfhi(res[v][1])};
        if (MODE == 1) a += (f32x4){bflo(bb[v][0]), bfhi(bb[v][0]), bflo(bb[v][1]), bfhi(bb[v][1])};
        const u32x2 o = {cvtpk(a[0], a[1]), cvtpk(a[2], a[3])};
        const int col = cb * 80 + v * 16 + 4 * g;
        *(u32x2*)((MODE == 0 ? (char*)(ws + WS_H) : out) + (size_t)tok * 1280 + col * 2) = o;
        if (MODE == 0) {
            const float h0 = bflo(o[0]), h1 = bfhi(o[0]), h2 = bflo(o[1]), h3 = bfhi(o[1]);
            s1 += (h0 + h1) + (h2 + h3); s2 += (h0 * h0 + h1 * h1) + (h2 * h2 + h3 * h3);
        }
    }
    if (MODE == 0) {
        s1 += __shfl_xor(s1, 16); s2 += __shfl_xor(s2, 16);
        s1 += __shfl_xor(s1, 32); s2 += __shfl_xor(s2, 32);
        if (g == 0) *(f32x2*)(ws + WS_ST + ((size_t)tok * 8 + cb) * 8) = (f32x2){s1, s2};
    }
}

extern "C" void kernel_launch(void* const* d_in, const int* in_sizes, int n_in, void* d_out, int out_size, void* d_ws, size_t ws_size,
                              hipStream_t stream) {
    const char* x = (const char*)d_in[0];
    const char* wqkv = (const char*)d_in[1];
    const char* wo = (const char*)d_in[2];
    const char* w1 = (const char*)d_in[4];
    const char* b1 = (const char*)d_in[5];
    const char* w2 = (const char*)d_in[6];
    const char* b2 = (const char*)d_in[7];
    char* ws = (char*)d_ws;
    (void)hipFuncSetAttribute((const void*)k_gemm128<0>, hipFuncAttributeMaxDynamicSharedMemorySize, G_LDS);
    (void)hipFuncSetAttribute((const void*)k_gemm128<1>, hipFuncAttributeMaxDynamicSharedMemorySize, G_LDS);
    (void)hipFuncSetAttribute((const void*)k_attn, hipFuncAttributeMaxDynamicSharedMemorySize, A_LDS);
    (void)hipFuncSetAttribute((const void*)k_gemm64<0>, hipFuncAttributeMaxDynamicSharedMemorySize, R_LDS);
    (void)hipFuncSetAttribute((const void*)k_gemm64<1>, hipFuncAttributeMaxDynamicSharedMemorySize, R_LDS);
    k_gemm128<0><<<256, 512, G_LDS, stream>>>(x, wqkv, nullptr, ws);
    k_attn<<<256, 512, A_LDS, stream>>>(wo, w2, ws);
    k_gemm64<0><<<256, 256, R_LDS, stream>>>(x, nullptr, ws, nullptr);
    k_gemm128<1><<<256, 512, G_LDS, stream>>>(nullptr, w1, b1, ws);
    k_gemm64<1><<<256, 256, R_LDS, stream>>>(nullptr, b2, ws, (char*)d_out);
}
```

```cpp
#include <hip/hip_runtime.h>
#include <hip/hip_bf16.h>
#include <stdint.h>

typedef __bf16 bf2 __attribute__((ext_vector_type(2)));
typedef __bf16 bf8 __attribute__((ext_vector_type(8)));
typedef short s16x4 __attribute__((ext_vector_type(4)));
typedef short s16x8 __attribute__((ext_vector_type(8)));
typedef float f32x2 __attribute__((ext_vector_type(2)));
typedef float f32x4 __attribute__((ext_vector_type(4)));
typedef float f32x16 __attribute__((ext_vector_type(16)));
typedef unsigned u32x2 __attribute__((ext_vector_type(2)));
typedef unsigned u32x4 __attribute__((ext_vector_type(4)));
#define LAS __attribute__((address_space(3)))
#define DEV __device__ __forceinline__
#define SBAR() __builtin_amdgcn_sched_barrier(0)

constexpr int S_ = 2048, D_ = 640, H_ = 32, F_ = 1728;
constexpr size_t WS_Q = 0;
constexpr size_t WS_KV = WS_Q + 32 * 64 * 1280;
constexpr size_t WS_KN = WS_KV + 32 * 64 * 2560;
constexpr size_t WS_O = WS_KN + 4096;
constexpr size_t WS_WO = WS_O + 128 * 80 * 256;
constexpr size_t WS_W2 = WS_WO + 40 * 20 * 1024;
constexpr size_t WS_H = WS_W2 + 40 * 54 * 1024;
constexpr size_t WS_ST = WS_H + 2048 * 640 * 2;
constexpr size_t WS_HID = WS_ST + 2048 * 8 * 8;
constexpr size_t WS_END = WS_HID + 128 * 216 * 256;

constexpr size_t WS_DIAG = 64u << 20;
#define STAMP(kid, idx) do {} while (0)
constexpr float QSCALE = 0.22360679774997896f * 1.4426950408889634f;
constexpr float LN_EPS = 1e-5f;

DEV unsigned cvtpk(float lo, float hi) { f32x2 v = {lo, hi}; return __builtin_bit_cast(unsigned, __builtin_convertvector(v, bf2)); }
DEV float bflo(unsigned u) { return __uint_as_float(u << 16); }
DEV float bfhi(unsigned u) { return __uint_as_float(u & 0xffff0000u); }
DEV float dot2(unsigned a, unsigned b, float c) { return __builtin_amdgcn_fdot2_f32_bf16(__builtin_bit_cast(bf2, a), __builtin_bit_cast(bf2, b), c, false); }
DEV f32x16 mfma32(u32x4 a, u32x4 b, f32x16 c) { return __builtin_amdgcn_mfma_f32_32x32x16_bf16(__builtin_bit_cast(bf8, a), __builtin_bit_cast(bf8, b), c, 0, 0, 0); }
DEV f32x16 mfma32k8(u32x2 a, u32x2 b, f32x16 c) { return __builtin_amdgcn_mfma_f32_32x32x8bf16_1k(__builtin_bit_cast(s16x4, a), __builtin_bit_cast(s16x4, b), c, 0, 0, 0); }
DEV f32x4 mfma16(u32x4 a, u32x4 b, f32x4 c) { return __builtin_amdgcn_mfma_f32_16x16x32_bf16(__builtin_bit_cast(bf8, a), __builtin_bit_cast(bf8, b), c, 0, 0, 0); }
DEV float swap32_add(float v) { auto r = __builtin_amdgcn_permlane32_swap(__float_as_uint(v), __float_as_uint(v), false, false); return __uint_as_float(r[0]) + __uint_as_float(r[1]); }
DEV float swap32_max(float v) { auto r = __builtin_amdgcn_permlane32_swap(__float_as_uint(v), __float_as_uint(v), false, false); return fmaxf(__uint_as_float(r[0]), __uint_as_float(r[1])); }
DEV float other32(float v, int hi) { auto r = __builtin_amdgcn_permlane32_swap(__float_as_uint(v), __float_as_uint(v), false, false); return __uint_as_float(hi ? r[0] : r[1]); }
DEV void glds16(unsigned voff, const void* sbase, unsigned ldsaddr) {
    unsigned sv;
    asm volatile("s_mov_b32 %0, m0\n\ts_mov_b32 m0, %3\n\ts_nop 0\n\tglobal_load_lds_dwordx4 %1, %2\n\ts_mov_b32 m0, %0"
                 : "=&s"(sv) : "v"(voff), "s"(sbase), "s"(ldsaddr) : "memory");
}
#define WAITV(n) asm volatile("s_waitcnt vmcnt(" #n ")" ::: "memory")
#define WAITL0() asm volatile("s_waitcnt lgkmcnt(0)" ::: "memory")
#define BAR() do { asm volatile("" ::: "memory"); __builtin_amdgcn_s_barrier(); asm volatile("" ::: "memory"); } while (0)
DEV u32x4 lds128(LAS const char* p) { return *(LAS const u32x4*)p; }
DEV u32x2 lds64(LAS const char* p) { return *(LAS const u32x2*)p; }
DEV u32x2 ldstr(LAS const char* p) { return __builtin_bit_cast(u32x2, __builtin_amdgcn_ds_read_tr16_b64_v4i16((LAS s16x4*)p)); }

constexpr int G_STAGE = 32768, G_RING = 4 * G_STAGE;
constexpr int G_STAT = G_RING;
constexpr int G_CS = G_STAT + 1024;
constexpr int G_NORM = G_CS + 512;
constexpr int G_LDS = G_NORM + 4096;

template <int WROW  >
DEV void g_loader(const char* xb, const char* wb, int l, int lane, int wcol  , unsigned lds0) {
    const int r8 = lane >> 3, pos = lane & 7;
    const unsigned xo0 = (unsigned)(r8 * 1280 + ((pos ^ ((lane >> 4) & 7)) * 16));
    const unsigned xo1 = (unsigned)(r8 * 1280 + ((pos ^ (4 + (lane >> 4))) * 16));
    const int r4 = lane >> 4, p16 = lane & 15;
    int cbyte = wcol + ((p16 ^ (r4 << 2)) * 16);
    if (cbyte >= WROW) cbyte -= 128;
    const unsigned wo = (unsigned)(r4 * WROW + cbyte);
    const char* xs = xb + (size_t)l * 32 * 1280;
    const char* ws = wb + (size_t)l * 16 * WROW;
    auto issue = [&](int s) {
        const unsigned dst = lds0 + (unsigned)(s & 3) * G_STAGE + (unsigned)l * 4096;
        const char* xp = xs + s * 128;
        const char* wp = ws + (size_t)s * 64 * WROW;
#pragma unroll
        for (int i = 0; i < 4; ++i) glds16((i & 1) ? xo1 : xo0, xp + i * 8 * 1280, dst + i * 1024);
#pragma unroll
        for (int i = 0; i < 4; ++i) glds16(wo, wp + (size_t)i * 4 * WROW, dst + 16384 + i * 1024);
    };
    issue(0); issue(1); issue(2); issue(3);
    WAITV(24); BAR();
#pragma unroll 1
    for (int s = 1; s < 7; ++s) { WAITV(16); BAR(); issue(s + 3); }
    WAITV(16); BAR();
    WAITV(8); BAR();
    WAITV(0); BAR();
}

template <bool VORI, bool XSTATS>
DEV void g_consumer(char* ws, LAS const char* lds, int ct, int cc, int lane, f32x16 (&acc)[2][2], float& sx, float& sxx, float& cs) {
    const int r = lane & 31, hh = lane >> 5;
    unsigned xa[2][4], wa[2];
    const int sw = (r >> 1) & 7;
#pragma unroll
    for (int ks = 0; ks < 4; ++ks) {
        xa[0][ks] = (unsigned)((2 * ct + cc) * 4096 + r * 128 + (((2 * ks) ^ hh ^ sw) * 16));
        xa[1][ks] = xa[0][ks] ^ 4096u;
    }
    {
        const int gam = lane >> 4, q = (lane & 15) >> 2, pp = lane & 3;
#pragma unroll
        for (int v = 0; v < 2; ++v) {
            const int T = 2 * cc + (v ^ ct);
            wa[v] = (unsigned)(16384 + (8 * (gam >> 1) + q) * 256 + (((T ^ q) * 4 + 2 * (gam & 1) + (pp >> 1)) * 16) + (pp & 1) * 8);
        }
    }
#pragma unroll
    for (int u = 0; u < 2; ++u)
#pragma unroll
        for (int v = 0; v < 2; ++v) acc[u][v] = f32x16{};
    sx = 0.f; sxx = 0.f; cs = 0.f;
    const unsigned ONES = 0x3f803f80u;
    u32x4 xf[2][2]; u32x2 wl[2][2], wh[2][2];
#define RD(set, boff, ks) do { \
        xf[set][0] = lds128(lds + (boff) + xa[0][ks]); xf[set][1] = lds128(lds + (boff) + xa[1][ks]); \
        wl[set][0] = ldstr(lds + (boff) + wa[0] + (ks) * 4096); wh[set][0] = ldstr(lds + (boff) + wa[0] + (ks) * 4096 + 1024); \
        wl[set][1] = ldstr(lds + (boff) + wa[1] + (ks) * 4096); wh[set][1] = ldstr(lds + (boff) + wa[1] + (ks) * 4096 + 1024); } while (0)
#define MM(set) do { \
        const u32x4 w0 = {wl[set][0][0], wl[set][0][1], wh[set][0][0], wh[set][0][1]}; \
        const u32x4 w1 = {wl[set][1][0], wl[set][1][1], wh[set][1][0], wh[set][1][1]}; \
        if (VORI) { acc[0][0] = mfma32(xf[set][0], w0, acc[0][0]); acc[0][1] = mfma32(xf[set][0], w1, acc[0][1]); \
                    acc[1][0] = mfma32(xf[set][1], w0, acc[1][0]); acc[1][1] = mfma32(xf[set][1], w1, acc[1][1]); } \
        else      { acc[0][0] = mfma32(w0, xf[set][0], acc[0][0]); acc[0][1] = mfma32(w1, xf[set][0], acc[0][1]); \
                    acc[1][0] = mfma32(w0, xf[set][1], acc[1][0]); acc[1][1] = mfma32(w1, xf[set][1], acc[1][1]); } \
        _Pragma("unroll") for (int j = 0; j < 4; ++j) { cs = dot2(w0[j], ONES, cs); \
            if (XSTATS) { sx = dot2(xf[set][0][j], ONES, sx); sxx = dot2(xf[set][0][j], xf[set][0][j], sxx); } } } while (0)
    BAR();
    if (ct == 0 && cc == 0) STAMP(XSTATS ? 0 : 3, 1);
    RD(0, 0, 0);
    unsigned boff = 0;
#pragma unroll 1
    for (int s = 0; s < 10; ++s) {
        RD(1, boff, 1); MM(0);
        RD(0, boff, 2); MM(1);
        RD(1, boff, 3); MM(0);
        if (s < 9) {
            WAITL0(); BAR();
            boff = (boff + G_STAGE) & (G_RING - 1);
            RD(0, boff, 0);
        }
        MM(1);
    }
#undef RD
#undef MM
}

template <int MODE>
__global__ __launch_bounds__(512, 2) void k_gemm128(const char* __restrict__ xin, const char* __restrict__ win,
                                                    const char* __restrict__ bias, char* __restrict__ ws) {
    extern __shared__ __attribute__((aligned(16))) char smem[];
    const int b = blockIdx.x, xcd = b & 7, i = b >> 3;
    int rb, cb;
    if (MODE == 0) { rb = (xcd & 1) * 8 + (i & 7); cb = (xcd >> 1) + 4 * (i >> 3); if (cb >= 15) return; }
    else           { rb = (xcd & 1) * 8 + (i & 7); cb = (xcd >> 1) + 4 * (i >> 3); if (cb >= 14) return; }
    const int tid = threadIdx.x, lane = tid & 63, wid = __builtin_amdgcn_readfirstlane(tid >> 6);
    const unsigned lds0 = (unsigned)(uintptr_t)smem;
    LAS char* lds = (LAS char*)smem;
    constexpr int WROW = MODE == 0 ? 3840 : 3456;
    const char* xb = (MODE == 0 ? xin : (const char*)(ws + WS_H)) + (size_t)rb * 128 * 1280;
    if (wid == 0) STAMP(MODE * 3, 0);
    if (wid >= 4) { g_loader<WROW>(xb, win, wid - 4, lane, cb * 256, lds0); if (wid == 4) STAMP(MODE * 3, 8); return; }

    const int ct = wid & 1, cc = wid >> 1, r = lane & 31, hi = lane >> 5;
    LAS f32x2* stat = (LAS f32x2*)(lds + G_STAT);
    LAS float* csum = (LAS float*)(lds + G_CS);
    LAS float* norm = (LAS float*)(lds + G_NORM);
    if (MODE == 1) {
        if (tid < 128) {
            const f32x4* p = (const f32x4*)(ws + WS_ST + (size_t)(rb * 128 + tid) * 64);
            const f32x4 a = p[0], bb = p[1], c = p[2], d = p[3];
            const float s1 = (a[0] + a[2]) + (bb[0] + bb[2]) + (c[0] + c[2]) + (d[0] + d[2]);
            const float s2 = (a[1] + a[3]) + (bb[1] + bb[3]) + (c[1] + c[3]) + (d[1] + d[3]);
            const float mu = s1 * (1.f / 640.f), var = fmaxf(s2 * (1.f / 640.f) - mu * mu, 0.f);
            stat[tid] = (f32x2){mu, 1.f / sqrtf(var + LN_EPS)};
        }
    } else {
        for (int k = tid; k < 1024; k += 256) norm[k] = 0.f;
    }
    f32x16 acc[2][2]; float sx, sxx, cs;
    const bool vori = (MODE == 0) && cb >= 10;
    if (vori) g_consumer<true, MODE == 0>(ws, lds, ct, cc, lane, acc, sx, sxx, cs);
    else g_consumer<false, MODE == 0>(ws, lds, ct, cc, lane, acc, sx, sxx, cs);

    if (wid == 0) STAMP(MODE * 3, 2);
    cs = swap32_add(cs);
    if (hi == 0) csum[(2 * cc + ct) * 32 + r] = cs;
    if (MODE == 0) {
        sx = swap32_add(sx); sxx = swap32_add(sxx);
        const float mu = sx * (1.f / 640.f), var = fmaxf(sxx * (1.f / 640.f) - mu * mu, 0.f);
        if (hi == 0) stat[(2 * ct + cc) * 32 + r] = (f32x2){mu, 1.f / sqrtf(var + LN_EPS)};
    }
    WAITL0(); BAR();

    if (MODE == 1) {
        const __hip_bfloat16* b1 = (const __hip_bfloat16*)bias;
#pragma unroll
        for (int v = 0; v < 2; ++v) {
            const int T = 2 * cc + (v ^ ct), cb32 = cb * 4 + T;
            if (cb32 >= 54) continue;
            f32x4 c4[4], b4[4];
#pragma unroll
            for (int a = 0; a < 4; ++a) {
                c4[a] = *(LAS const f32x4*)(csum + T * 32 + 8 * a + 4 * hi);
                const u32x2 bb = *(const u32x2*)((const char*)b1 + (cb32 * 32 + 8 * a + 4 * hi) * 2);
                b4[a] = (f32x4){bflo(bb[0]), bfhi(bb[0]), bflo(bb[1]), bfhi(bb[1])};
            }
#pragma unroll
            for (int u = 0; u < 2; ++u) {
                const int tt = 2 * ct + (u ^ cc);
                const f32x2 st = stat[tt * 32 + r];
                float h[16];
#pragma unroll
                for (int k = 0; k < 16; ++k) {
                    const float z = fmaf(fmaf(-st[0], c4[k >> 2][k & 3], acc[u][v][k]), st[1], b4[k >> 2][k & 3]);
                    const float rl = fmaxf(z, 0.f);
                    h[k] = rl * rl;
                }
                const int tok = rb * 128 + tt * 32 + r;
#pragma unroll
                for (int s = 0; s < 2; ++s) {
                    const u32x4 w = {cvtpk(h[8 * s], h[8 * s + 1]), cvtpk(h[8 * s + 2], h[8 * s + 3]), cvtpk(h[8 * s + 4], h[8 * s + 5]), cvtpk(h[8 * s + 6], h[8 * s + 7])};
                    const int q = (cb32 * 2 + s) * 2 + hi;
                    *(u32x4*)(ws + WS_HID + ((size_t)((tok >> 4) * 216 + q) * 16 + (tok & 15)) * 16) = w;
                }
            }
        }
        if (wid == 0) STAMP(3, 3);
        return;
    }
    if (vori) {
#pragma unroll
        for (int u = 0; u < 2; ++u) {
            const int tt = 2 * ct + (u ^ cc);
            f32x2 st[16];
#pragma unroll
            for (int a = 0; a < 4; ++a) {
                const f32x4 s0 = *(LAS const f32x4*)(stat + tt * 32 + 8 * a + 4 * hi), s1 = *(LAS const f32x4*)(stat + tt * 32 + 8 * a + 4 * hi + 2);
                st[4 * a] = (f32x2){s0[0], s0[1]}; st[4 * a + 1] = (f32x2){s0[2], s0[3]};
                st[4 * a + 2] = (f32x2){s1[0], s1[1]}; st[4 * a + 3] = (f32x2){s1[2], s1[3]};
            }
#pragma unroll
            for (int v = 0; v < 2; ++v) {
                const int T = 2 * cc + (v ^ ct);
                const int col = (cb - 10) * 128 + T * 32 + r, head = col / 20, d = col - head * 20;
                const float c = csum[T * 32 + r];
                float h[16];
#pragma unroll
                for (int k = 0; k < 16; ++k) h[k] = fmaf(-st[k][0], c, acc[u][v][k]) * st[k][1];
                char* rec = ws + WS_KV + ((size_t)head * 64 + rb * 4 + tt) * 2560 + 1280 + hi * 320 + d * 16;
#pragma unroll
                for (int s = 0; s < 2; ++s) {
                    const u32x4 w = {cvtpk(h[8 * s], h[8 * s + 1]), cvtpk(h[8 * s + 2], h[8 * s + 3]), cvtpk(h[8 * s + 4], h[8 * s + 5]), cvtpk(h[8 * s + 6], h[8 * s + 7])};
                    *(u32x4*)(rec + s * 640) = w;
                }
            }
        }
        if (wid == 0) STAMP(0, 3);
        return;
    }
    const bool isK = cb >= 5;
    const int cbl = isK ? cb - 5 : cb;
    const int head0 = (32 * cbl) / 5;
#pragma unroll
    for (int v = 0; v < 2; ++v) {
        const int T = 2 * cc + (v ^ ct);
        f32x4 c4[4];
#pragma unroll
        for (int a = 0; a < 4; ++a) c4[a] = *(LAS const f32x4*)(csum + T * 32 + 8 * a + 4 * hi);
#pragma unroll
        for (int u = 0; u < 2; ++u) {
            const int tt = 2 * ct + (u ^ cc);
            const f32x2 st = stat[tt * 32 + r];
            const float sc = isK ? st[1] : st[1] * QSCALE;
#pragma unroll
            for (int a = 0; a < 4; ++a) {
                float h[4];
#pragma unroll
                for (int k = 0; k < 4; ++k) h[k] = fmaf(-st[0], c4[a][k], acc[u][v][4 * a + k]) * sc;
                const int gg = 32 * cbl + T * 8 + 2 * a + hi, head = gg / 5, g = gg - head * 5;
                const u32x2 w = {cvtpk(h[0], h[1]), cvtpk(h[2], h[3])};
                char* rec = isK ? ws + WS_KV + ((size_t)head * 64 + rb * 4 + tt) * 2560 : ws + WS_Q + ((size_t)head * 64 + rb * 4 + tt) * 1280;
                const int off = g < 4 ? (g >> 1) * 512 + r * 16 + (g & 1) * 8 : 1024 + r * 8;
                *(u32x2*)(rec + off) = w;
                if (isK) {
                    const float n2 = (h[0] * h[0] + h[1] * h[1]) + (h[2] * h[2] + h[3] * h[3]);
                    __hip_atomic_fetch_add(norm + (head - head0) * 128 + tt * 32 + r, n2, __ATOMIC_RELAXED, __HIP_MEMORY_SCOPE_WORKGROUP);
                }
            }
        }
    }
    if (isK) {
        WAITL0(); BAR();
#pragma unroll
        for (int hl = 0; hl < 2; ++hl) {
            const int lh = wid * 2 + hl;
            float m = fmaxf(norm[lh * 128 + lane], norm[lh * 128 + 64 + lane]);
#pragma unroll
            for (int o = 32; o >= 1; o >>= 1) m = fmaxf(m, __shfl_xor(m, o));
            if (lane == 0) ((float*)(ws + WS_KN))[(rb * 5 + cbl) * 8 + lh] = m;
        }
    }
    if (wid == 0) STAMP(0, 3);
}

constexpr int A_STAGE = 40960, A_RING = 3 * A_STAGE;
constexpr int A_ONES = A_RING;
constexpr int A_FLAG = A_ONES + 8 * 2560;
constexpr int A_LDS = A_FLAG + 64;

DEV void prep_addr(int task, int lane, const char* wo, const char* w2, const char*& base, unsigned& o0, unsigned& o1, unsigned& dst) {
    const int col = lane & 15, g = lane >> 4;
    if (task < 800) {
        const int ct = task % 40, ks = task / 40;
        const int q = 4 * ks + g, p = q / 5, c = q - 5 * p;
        int k0, k1;
        if (c < 4) { k0 = (2 * p + (c >> 1)) * 20 + 4 * (c & 1); k1 = k0 + 8; }
        else { k0 = (2 * p) * 20 + 16; k1 = k0 + 20; }
        base = wo; o0 = (unsigned)(k0 * 1280 + (ct * 16 + col) * 2); o1 = (unsigned)(k1 * 1280 + (ct * 16 + col) * 2);
        dst = (unsigned)(WS_WO + (size_t)(ct * 20 + ks) * 1024 + lane * 16);
    } else {
        const int t = task - 800, ct = t % 40, ks = t / 40;
        const int k0 = 32 * ks + 16 * (g >> 1) + 4 * (g & 1);
        base = w2; o0 = (unsigned)(k0 * 1280 + (ct * 16 + col) * 2); o1 = o0 + 8 * 1280;
        dst = (unsigned)(WS_W2 + (size_t)(ct * 54 + ks) * 1024 + lane * 16);
    }
}
DEV void prep_task(int task, int lane, const char* wo, const char* w2, char* ws) {
    const char* base; unsigned o0, o1, dst;
    prep_addr(task, lane, wo, w2, base, o0, o1, dst);
    unsigned short e[8];
#pragma unroll
    for (int j = 0; j < 4; ++j) { e[j] = *(const unsigned short*)(base + o0 + j * 1280); e[4 + j] = *(const unsigned short*)(base + o1 + j * 1280); }
    const u32x4 w = {e[0] | ((unsigned)e[1] << 16), e[2] | ((unsigned)e[3] << 16), e[4] | ((unsigned)e[5] << 16), e[6] | ((unsigned)e[7] << 16)};
    *(u32x4*)(ws + dst) = w;
}

__global__ __launch_bounds__(512, 2) void k_attn(const char* __restrict__ wo, const char* __restrict__ w2, char* __restrict__ ws) {
    extern __shared__ __attribute__((aligned(16))) char smem[];
    const int b = blockIdx.x, xcd = b & 7, i = b >> 3;
    const int p = 2 * xcd + (i >> 4), rb = i & 15;
    const int tid = threadIdx.x, lane = tid & 63, wid = __builtin_amdgcn_readfirstlane(tid >> 6);
    const int hp = wid >> 2, head = 2 * p + hp, qt = rb * 4 + (wid & 3);
    const int r = lane & 31, hi = lane >> 5;
    const unsigned lds0 = (unsigned)(uintptr_t)smem;
    LAS char* lds = (LAS char*)smem;

    if (wid == 0) STAMP(1, 0);
    u32x4 q1; u32x2 q2; float knv;
    const int kb0 = (5 * head) / 32, kb1 = (5 * head + 4) / 32;
    {
        const char* qb = ws + WS_Q + ((size_t)head * 64 + qt) * 1280;
        const unsigned o1 = lane * 16, o2 = 1024 + r * 8;
        const int kb = (lane & 16) ? kb1 : kb0;
        const unsigned o3 = (unsigned)((((lane & 15) * 5 + kb) * 8 + head - (32 * kb) / 5) * 4);
        const char* knb = ws + WS_KN;
        asm volatile("global_load_dwordx4 %0, %3, %6\n\tglobal_load_dwordx2 %1, %4, %6\n\tglobal_load_dword %2, %5, %7"
                     : "=&v"(q1), "=&v"(q2), "=&v"(knv) : "v"(o1), "v"(o2), "v"(o3), "s"(qb), "s"(knb) : "memory");
    }
    const char* kvb = ws + WS_KV + (size_t)head * 64 * 2560 + (wid & 3) * 5120;
    const unsigned dsto = lds0 + hp * 20480 + (wid & 3) * 5120;
    const unsigned lo16 = lane * 16;
    auto issue = [&](int s, int buf) {
#pragma unroll
        for (int k = 0; k < 5; ++k) glds16(lo16, kvb + (size_t)s * 20480 + k * 1024, dsto + buf * A_STAGE + k * 1024);
    };
    issue(0, 0); issue(1, 1); issue(2, 2);
    const int gw = b * 8 + wid;
    if (tid < 16) *(LAS u32x4*)(lds + A_ONES + (tid >> 1) * 2560 + (tid & 1) * 640 + 128) = (u32x4){0x3f803f80u, 0x3f803f80u, 0x3f803f80u, 0x3f803f80u};
    if (tid == 0) *(LAS unsigned*)(lds + A_FLAG) = 0;
    asm volatile("s_waitcnt vmcnt(10)" : "+v"(q1), "+v"(q2), "+v"(knv) : : "memory");
    if ((lane & 16) && kb1 == kb0) knv = 0.f;
#pragma unroll
    for (int o_ = 1; o_ < 16; o_ <<= 1) knv = fmaxf(knv, __shfl_xor(knv, o_));
    const float kn2 = knv + __shfl_xor(knv, 16);
    if (hi) q2 = (u32x2){0, 0};
    float qn2 = 0.f;
#pragma unroll
    for (int k = 0; k < 4; ++k) qn2 = dot2(q1[k], q1[k], qn2);
    qn2 = dot2(q2[0], q2[0], qn2); qn2 = dot2(q2[1], q2[1], qn2);
    qn2 = swap32_add(qn2);
    const float mq = sqrtf(qn2 * kn2) * 1.02f + 1e-3f;
    const bool unsafe = !(mq <= 50.f);
    WAITL0(); BAR();
    if (__any(unsafe) && lane == 0) *(LAS unsigned*)(lds + A_FLAG) = 1;
    WAITL0(); BAR();
    const bool slow = __builtin_amdgcn_readfirstlane(*(LAS const unsigned*)(lds + A_FLAG)) != 0;

    if (wid == 0) STAMP(1, 1);
    const unsigned ka = hp * 20480 + lane * 16;
    const unsigned k2a = hp * 20480 + 1024 + r * 8;
    unsigned va = hp * 20480 + 1280 + hi * 320 + r * 16;
    const bool vdata = r < 20 || r >= 28;
    if (r >= 28) va = hp * 20480 + 1280 + hi * 320 + 16 * 16;
    if (!vdata) va = A_ONES + 128;
    f32x16 o = f32x16{};

    if (!slow) {
        f32x16 negm;
#pragma unroll
        for (int k = 0; k < 16; ++k) negm[k] = -mq;
        unsigned kp = ka, k2p = k2a, vp = va;
        u32x4 k1f, v0f, v1f, p0, p1; u32x2 k2f;
        f32x16 sa, sb;
        k1f = lds128(lds + kp); k2f = lds64(lds + k2p);
        sa = mfma32(k1f, q1, negm); sa = mfma32k8(k2f, q2, sa);
        k1f = lds128(lds + kp + 2560); k2f = lds64(lds + k2p + 2560);
        v0f = lds128(lds + vp);
        v1f = (u32x4){0, 0, 0, 0}; p1 = (u32x4){0, 0, 0, 0};
#define EXP4(S, k) do { S[k] = __builtin_amdgcn_exp2f(S[k]); S[k + 1] = __builtin_amdgcn_exp2f(S[k + 1]); S[k + 2] = __builtin_amdgcn_exp2f(S[k + 2]); S[k + 3] = __builtin_amdgcn_exp2f(S[k + 3]); } while (0)
#define PIN(x) asm volatile("" : "+v"(x))
#define TILE(S, N, KP, K2P, KO, VP1, VO1, VP0, VO0) do { \
        N = mfma32(k1f, q1, negm); k1f = lds128(lds + (KP) + (KO)); \
        EXP4(S, 0); p0[0] = cvtpk(S[0], S[1]); p0[1] = cvtpk(S[2], S[3]); PIN(p0); SBAR(); \
        o = mfma32(v1f, p1, o); v1f = lds128(lds + (VP1) + (VO1) + 640); \
        EXP4(S, 4); p0[2] = cvtpk(S[4], S[5]); p0[3] = cvtpk(S[6], S[7]); PIN(p0); SBAR(); \
        N = mfma32k8(k2f, q2, N); k2f = lds64(lds + (K2P) + (KO)); \
        EXP4(S, 8); p1[0] = cvtpk(S[8], S[9]); p1[1] = cvtpk(S[10], S[11]); PIN(p1); SBAR(); \
        o = mfma32(v0f, p0, o); v0f = lds128(lds + (VP0) + (VO0)); \
        EXP4(S, 12); p1[2] = cvtpk(S[12], S[13]); p1[3] = cvtpk(S[14], S[15]); PIN(p1); SBAR(); } while (0)
        int buf = 0;
#pragma unroll 1
        for (int s = 0; s < 8; ++s) {
            TILE(sa, sb, kp, k2p, 2 * 2560, vp, 0 * 2560, vp, 1 * 2560);
            TILE(sb, sa, kp, k2p, 3 * 2560, vp, 1 * 2560, vp, 2 * 2560);
            TILE(sa, sb, kp, k2p, 4 * 2560, vp, 2 * 2560, vp, 3 * 2560);
            TILE(sb, sa, kp, k2p, 5 * 2560, vp, 3 * 2560, vp, 4 * 2560);
            TILE(sa, sb, kp, k2p, 6 * 2560, vp, 4 * 2560, vp, 5 * 2560);
            TILE(sb, sa, kp, k2p, 7 * 2560, vp, 5 * 2560, vp, 6 * 2560);
            const int nb = buf == 2 ? 0 : buf + 1;
            const unsigned kn = ka + nb * A_STAGE, k2n = k2a + nb * A_STAGE, vn = va + (vdata ? nb * A_STAGE : 0);
            if (s < 7) {
                WAITV(0); WAITL0(); BAR();
                if (s >= 1 && s < 6) issue(s + 2, buf == 0 ? 2 : buf - 1);
                if (s == 1 + 2 * hp) {
                    prep_task(gw, lane, wo, w2, ws);
                    if (gw + 2048 < 2960) prep_task(gw + 2048, lane, wo, w2, ws);
                }
            }
            TILE(sa, sb, kn, k2n, 0, vp, 6 * 2560, vp, 7 * 2560);
            TILE(sb, sa, kn, k2n, 2560, vp, 7 * 2560, vn, 0);
            kp = kn; k2p = k2n; vp = vn; buf = nb;
        }
        o = mfma32(v1f, p1, o);
    } else {
        float m = -INFINITY;
        int buf = 0;
#pragma unroll 1
        for (int s = 0; s < 8; ++s) {
            if (s > 0) {
                WAITV(0); WAITL0(); BAR();
                if (s >= 1 && s < 6) issue(s + 2, buf == 0 ? 2 : buf - 1);
                if (s == 1) {
                    prep_task(gw, lane, wo, w2, ws);
                    if (gw + 2048 < 2960) prep_task(gw + 2048, lane, wo, w2, ws);
                }
            }
            const unsigned so = buf * A_STAGE, vo = vdata ? so : 0;
#pragma unroll 1
            for (int t = 0; t < 8; ++t) {
                const u32x4 k1f = lds128(lds + ka + so + t * 2560); const u32x2 k2f = lds64(lds + k2a + so + t * 2560);
                const u32x4 v0f = lds128(lds + va + vo + t * 2560), v1f = lds128(lds + va + vo + t * 2560 + 640);
                f32x16 sc = mfma32(k1f, q1, f32x16{}); sc = mfma32k8(k2f, q2, sc);
                float tm = sc[0];
#pragma unroll
                for (int k = 1; k < 16; ++k) tm = fmaxf(tm, sc[k]);
                tm = swap32_max(tm);
                const float mn = fmaxf(m, tm), al = __builtin_amdgcn_exp2f(m - mn);
                m = mn;
#pragma unroll
                for (int k = 0; k < 16; ++k) { o[k] *= al; sc[k] = __builtin_amdgcn_exp2f(sc[k] - mn); }
                const u32x4 p0 = {cvtpk(sc[0], sc[1]), cvtpk(sc[2], sc[3]), cvtpk(sc[4], sc[5]), cvtpk(sc[6], sc[7])};
                const u32x4 p1 = {cvtpk(sc[8], sc[9]), cvtpk(sc[10], sc[11]), cvtpk(sc[12], sc[13]), cvtpk(sc[14], sc[15])};
                o = mfma32(v0f, p0, o); o = mfma32(v1f, p1, o);
            }
            buf = buf == 2 ? 0 : buf + 1;
        }
    }
    if (wid == 0) STAMP(1, 2);
    const float lo_ = other32(o[8], hi);
    const float l = hi ? o[8] : lo_;
    const float il = 1.f / l;
    const int tok = qt * 32 + r;
    char* ob = ws + WS_O + ((size_t)((tok >> 4) * 80 + 5 * p) * 16 + (tok & 15)) * 16;
    const u32x4 w = {cvtpk(o[0] * il, o[1] * il), cvtpk(o[2] * il, o[3] * il), cvtpk(o[4] * il, o[5] * il), cvtpk(o[6] * il, o[7] * il)};
    *(u32x4*)(ob + (2 * hp + hi) * 256) = w;
    if (hi == 0) *(u32x2*)(ob + 4 * 256 + hp * 8) = (u32x2){cvtpk(o[8] * il, o[9] * il), cvtpk(o[10] * il, o[11] * il)};
    if (wid == 0) STAMP(1, 3);
}

constexpr int R_LDS = 61440;
template <int MODE>
__global__ __launch_bounds__(256, 1) void k_gemm64(const char* __restrict__ xin, const char* __restrict__ bias, char* __restrict__ ws, char* __restrict__ out) {
    extern __shared__ __attribute__((aligned(16))) char smem[];
    constexpr int NK = MODE == 0 ? 20 : 54, NCH = 4 * NK, DEPTH = MODE == 0 ? 5 : 6, NMAX = (NK + 3) / 4;
    const int b = blockIdx.x, xcd = b & 7, i = b >> 3;
    const int rb = (xcd >> 1) * 8 + (i & 7), cb = (xcd & 1) * 4 + (i >> 3);
    const int tid = threadIdx.x, lane = tid & 63, w = __builtin_amdgcn_readfirstlane(tid >> 6);
    if (w == 0) STAMP(MODE == 0 ? 2 : 4, 0);
    const char* act = ws + (MODE == 0 ? WS_O : WS_HID) + (size_t)lane * 16;
    const char* wf = ws + (MODE == 0 ? WS_WO : WS_W2) + (size_t)cb * 5 * NK * 1024 + (size_t)lane * 16;
    const char* ap[4];
#pragma unroll
    for (int u = 0; u < 4; ++u) ap[u] = act + (size_t)(rb * 4 + ((u + w) & 3)) * NCH * 256;
    f32x4 acc[4][5];
#pragma unroll
    for (int u = 0; u < 4; ++u)
#pragma unroll
        for (int v = 0; v < 5; ++v) acc[u][v] = f32x4{};
    u32x4 af[DEPTH][4], bf[DEPTH][5];
    const int nmine = (NK - w + 3) / 4;
#define LOADSTEP(slot, n) do { const int ks_ = w + 4 * (n); \
        _Pragma("unroll") for (int v = 0; v < 5; ++v) bf[slot][v] = *(const u32x4*)(wf + ((size_t)v * NK + ks_) * 1024); \
        _Pragma("unroll") for (int u = 0; u < 4; ++u) af[slot][u] = *(const u32x4*)(ap[u] + (size_t)ks_ * 1024); } while (0)
#pragma unroll
    for (int n = 0; n < DEPTH; ++n) LOADSTEP(n, n);
    SBAR();
#pragma unroll
    for (int n = 0; n < NMAX; ++n) {
        if (n < NK / 4 || n < nmine) {
#pragma unroll
            for (int u = 0; u < 4; ++u)
#pragma unroll
                for (int v = 0; v < 5; ++v) acc[u][v] = mfma16(bf[n % DEPTH][v], af[n % DEPTH][u], acc[u][v]);
            SBAR();
            if (n + DEPTH < NMAX && (n + DEPTH < NK / 4 || n + DEPTH < nmine)) LOADSTEP(n % DEPTH, n + DEPTH);
            SBAR();
        }
    }
#undef LOADSTEP
    if (w == 0) STAMP(MODE == 0 ? 2 : 4, 2);
    LAS char* lds = (LAS char*)smem;
#pragma unroll
    for (int u = 1; u < 4; ++u)
#pragma unroll
        for (int v = 0; v < 5; ++v) *(LAS f32x4*)(lds + ((((u + w) & 3) * 3 + (u - 1)) * 5 + v) * 1024 + lane * 16) = acc[u][v];
    const int tk = lane & 15, g = lane >> 4, tok = rb * 64 + w * 16 + tk;
    u32x2 res[5], bb[5];
#pragma unroll
    for (int v = 0; v < 5; ++v) {
        const int col = cb * 80 + v * 16 + 4 * g;
        res[v] = *(const u32x2*)((MODE == 0 ? xin : (const char*)(ws + WS_H)) + (size_t)tok * 1280 + col * 2);
        if (MODE == 1) bb[v] = *(const u32x2*)(bias + col * 2);
    }
    WAITL0(); BAR();
    float s1 = 0.f, s2 = 0.f;
#pragma unroll
    for (int v = 0; v < 5; ++v) {
        f32x4 a = acc[0][v];
#pragma unroll
        for (int k = 0; k < 3; ++k) a += *(LAS const f32x4*)(lds + ((w * 3 + k) * 5 + v) * 1024 + lane * 16);
        a += (f32x4){bflo(res[v][0]), bfhi(res[v][0]), bflo(res[v][1]), bfhi(res[v][1])};
        if (MODE == 1) a += (f32x4){bflo(bb[v][0]), bfhi(bb[v][0]), bflo(bb[v][1]), bfhi(bb[v][1])};
        const u32x2 o = {cvtpk(a[0], a[1]), cvtpk(a[2], a[3])};
        const int col = cb * 80 + v * 16 + 4 * g;
        *(u32x2*)((MODE == 0 ? (char*)(ws + WS_H) : out) + (size_t)tok * 1280 + col * 2) = o;
        if (MODE == 0) {
            const float h0 = bflo(o[0]), h1 = bfhi(o[0]), h2 = bflo(o[1]), h3 = bfhi(o[1]);
            s1 += (h0 + h1) + (h2 + h3); s2 += (h0 * h0 + h1 * h1) + (h2 * h2 + h3 * h3);
        }
    }
    if (MODE == 0) {
        s1 += __shfl_xor(s1, 16); s2 += __shfl_xor(s2, 16);
        s1 += __shfl_xor(s1, 32); s2 += __shfl_xor(s2, 32);
        if (g == 0) *(f32x2*)(ws + WS_ST + ((size_t)tok * 8 + cb) * 8) = (f32x2){s1, s2};
    }
    if (w == 0) STAMP(MODE == 0 ? 2 : 4, 3);
}

extern "C" void kernel_launch(void* const* d_in, const int* in_sizes, int n_in, void* d_out, int out_size, void* d_ws, size_t ws_size,
                              hipStream_t stream) {
    const char* x = (const char*)d_in[0];
    const char* wqkv = (const char*)d_in[1];
    const char* wo = (const char*)d_in[2];
    const char* w1 = (const char*)d_in[4];
    const char* b1 = (const char*)d_in[5];
    const char* w2 = (const char*)d_in[6];
    const char* b2 = (const char*)d_in[7];
    char* ws = (char*)d_ws;
    (void)hipFuncSetAttribute((const void*)k_gemm128<0>, hipFuncAttributeMaxDynamicSharedMemorySize, G_LDS);
    (void)hipFuncSetAttribute((const void*)k_gemm128<1>, hipFuncAttributeMaxDynamicSharedMemorySize, G_LDS);
    (void)hipFuncSetAttribute((const void*)k_attn, hipFuncAttributeMaxDynamicSharedMemorySize, A_LDS);
    (void)hipFuncSetAttribute((const void*)k_gemm64<0>, hipFuncAttributeMaxDynamicSharedMemorySize, R_LDS);
    (void)hipFuncSetAttribute((const void*)k_gemm64<1>, hipFuncAttributeMaxDynamicSharedMemorySize, R_LDS);
    k_gemm128<0><<<256, 512, G_LDS, stream>>>(x, wqkv, nullptr, ws);
    k_attn<<<256, 512, A_LDS, stream>>>(wo, w2, ws);
    k_gemm64<0><<<256, 256, R_LDS, stream>>>(x, nullptr, ws, nullptr);
    k_gemm128<1><<<256, 512, G_LDS, stream>>>(nullptr, w1, b1, ws);
    k_gemm64<1><<<256, 256, R_LDS, stream>>>(nullptr, b2, ws, (char*)d_out);
}
```

```cpp
#include <hip/hip_runtime.h>
#include <hip/hip_bf16.h>
#include <stdint.h>

typedef __bf16 bf16x2_t __attribute__((ext_vector_type(2)));
typedef __bf16 bf16x8_t __attribute__((ext_vector_type(8)));
typedef short s16x4 __attribute__((ext_vector_type(4)));
typedef float f32x2 __attribute__((ext_vector_type(2)));
typedef float f32x4 __attribute__((ext_vector_type(4)));
typedef float f32x16 __attribute__((ext_vector_type(16)));
typedef unsigned u32x2 __attribute__((ext_vector_type(2)));
typedef unsigned u32x4 __attribute__((ext_vector_type(4)));
#define LAS __attribute__((address_space(3)))
#define DEV __device__ __forceinline__
#define SBAR() __builtin_amdgcn_sched_barrier(0)

#define ATTN_LOOP \
    "s_add_u32 m0, %[slot], 2048\n\t" \
    "1:\n\t" \
    "s_waitcnt lgkmcnt(2)\n\t" \
    "v_mfma_f32_32x32x16_bf16 v[32:47], v[70:73], v[64:67], v[48:63]\n\t" \
    "ds_read_b128 v[70:73], v100 offset:10240\n\t" \
    "v_cvt_pk_bf16_f32 v90, v96, v97\n\t" \
    "v_cvt_pk_bf16_f32 v91, v98, v99\n\t" \
    "v_exp_f32 v92, v16\n\t" \
    "v_exp_f32 v93, v17\n\t" \
    "v_exp_f32 v94, v18\n\t" \
    "v_exp_f32 v95, v19\n\t" \
    "v_mfma_f32_32x32x16_bf16 v[0:15], v[80:83], v[88:91], v[0:15]\n\t" \
    "ds_read_b128 v[80:83], v103 offset:640\n\t" \
    "global_load_lds_dwordx4 v105, %[ws]\n\t" \
    "v_cvt_pk_bf16_f32 v84, v92, v93\n\t" \
    "v_cvt_pk_bf16_f32 v85, v94, v95\n\t" \
    "v_exp_f32 v96, v20\n\t" \
    "v_exp_f32 v97, v21\n\t" \
    "v_exp_f32 v98, v22\n\t" \
    "v_exp_f32 v99, v23\n\t" \
    "s_waitcnt lgkmcnt(2)\n\t" \
    "v_mfma_f32_32x32x8_bf16 v[32:47], v[74:75], v[68:69], v[32:47]\n\t" \
    "ds_read_b64 v[74:75], v102 offset:10240\n\t" \
    "v_cvt_pk_bf16_f32 v86, v96, v97\n\t" \
    "v_cvt_pk_bf16_f32 v87, v98, v99\n\t" \
    "v_exp_f32 v92, v24\n\t" \
    "v_exp_f32 v93, v25\n\t" \
    "v_exp_f32 v94, v26\n\t" \
    "v_exp_f32 v95, v27\n\t" \
    "v_mfma_f32_32x32x16_bf16 v[0:15], v[76:79], v[84:87], v[0:15]\n\t" \
    "ds_read_b128 v[76:79], v103 offset:1280\n\t" \
    "v_cvt_pk_bf16_f32 v88, v92, v93\n\t" \
    "v_cvt_pk_bf16_f32 v89, v94, v95\n\t" \
    "v_exp_f32 v96, v28\n\t" \
    "v_exp_f32 v97, v29\n\t" \
    "v_exp_f32 v98, v30\n\t" \
    "v_exp_f32 v99, v31\n\t" \
    "s_waitcnt lgkmcnt(2)\n\t" \
    "v_mfma_f32_32x32x16_bf16 v[16:31], v[70:73], v[64:67], v[48:63]\n\t" \
    "ds_read_b128 v[70:73], v100 offset:10752\n\t" \
    "v_cvt_pk_bf16_f32 v90, v96, v97\n\t" \
    "v_cvt_pk_bf16_f32 v91, v98, v99\n\t" \
    "v_exp_f32 v92, v32\n\t" \
    "v_exp_f32 v93, v33\n\t" \
    "v_exp_f32 v94, v34\n\t" \
    "v_exp_f32 v95, v35\n\t" \
    "v_mfma_f32_32x32x16_bf16 v[0:15], v[80:83], v[88:91], v[0:15]\n\t" \
    "ds_read_b128 v[80:83], v103 offset:1920\n\t" \
    "global_load_lds_dwordx4 v105, %[ws] offset:1024\n\t" \
    "v_cvt_pk_bf16_f32 v84, v92, v93\n\t" \
    "v_cvt_pk_bf16_f32 v85, v94, v95\n\t" \
    "v_exp_f32 v96, v36\n\t" \
    "v_exp_f32 v97, v37\n\t" \
    "v_exp_f32 v98, v38\n\t" \
    "v_exp_f32 v99, v39\n\t" \
    "s_waitcnt lgkmcnt(2)\n\t" \
    "v_mfma_f32_32x32x8_bf16 v[16:31], v[74:75], v[68:69], v[16:31]\n\t" \
    "ds_read_b64 v[74:75], v102 offset:10752\n\t" \
    "v_cvt_pk_bf16_f32 v86, v96, v97\n\t" \
    "v_cvt_pk_bf16_f32 v87, v98, v99\n\t" \
    "v_exp_f32 v92, v40\n\t" \
    "v_exp_f32 v93, v41\n\t" \
    "v_exp_f32 v94, v42\n\t" \
    "v_exp_f32 v95, v43\n\t" \
    "v_mfma_f32_32x32x16_bf16 v[0:15], v[76:79], v[84:87], v[0:15]\n\t" \
    "ds_read_b128 v[76:79], v103 offset:10240\n\t" \
    "v_cvt_pk_bf16_f32 v88, v92, v93\n\t" \
    "v_cvt_pk_bf16_f32 v89, v94, v95\n\t" \
    "v_exp_f32 v96, v44\n\t" \
    "v_exp_f32 v97, v45\n\t" \
    "v_exp_f32 v98, v46\n\t" \
    "v_exp_f32 v99, v47\n\t" \
    "s_waitcnt lgkmcnt(2)\n\t" \
    "v_mfma_f32_32x32x16_bf16 v[32:47], v[70:73], v[64:67], v[48:63]\n\t" \
    "ds_read_b128 v[70:73], v100 offset:20480\n\t" \
    "v_cvt_pk_bf16_f32 v90, v96, v97\n\t" \
    "v_cvt_pk_bf16_f32 v91, v98, v99\n\t" \
    "v_exp_f32 v92, v16\n\t" \
    "v_exp_f32 v93, v17\n\t" \
    "v_exp_f32 v94, v18\n\t" \
    "v_exp_f32 v95, v19\n\t" \
    "v_mfma_f32_32x32x16_bf16 v[0:15], v[80:83], v[88:91], v[0:15]\n\t" \
    "ds_read_b128 v[80:83], v103 offset:10880\n\t" \
    "global_load_lds_dwordx4 v105, %[ws] offset:2048\n\t" \
    "v_cvt_pk_bf16_f32 v84, v92, v93\n\t" \
    "v_cvt_pk_bf16_f32 v85, v94, v95\n\t" \
    "v_exp_f32 v96, v20\n\t" \
    "v_exp_f32 v97, v21\n\t" \
    "v_exp_f32 v98, v22\n\t" \
    "v_exp_f32 v99, v23\n\t" \
    "s_waitcnt lgkmcnt(2)\n\t" \
    "v_mfma_f32_32x32x8_bf16 v[32:47], v[74:75], v[68:69], v[32:47]\n\t" \
    "ds_read_b64 v[74:75], v102 offset:20480\n\t" \
    "v_cvt_pk_bf16_f32 v86, v96, v97\n\t" \
    "v_cvt_pk_bf16_f32 v87, v98, v99\n\t" \
    "v_exp_f32 v92, v24\n\t" \
    "v_exp_f32 v93, v25\n\t" \
    "v_exp_f32 v94, v26\n\t" \
    "v_exp_f32 v95, v27\n\t" \
    "v_mfma_f32_32x32x16_bf16 v[0:15], v[76:79], v[84:87], v[0:15]\n\t" \
    "ds_read_b128 v[76:79], v103 offset:11520\n\t" \
    "v_cvt_pk_bf16_f32 v88, v92, v93\n\t" \
    "v_cvt_pk_bf16_f32 v89, v94, v95\n\t" \
    "v_exp_f32 v96, v28\n\t" \
    "v_exp_f32 v97, v29\n\t" \
    "v_exp_f32 v98, v30\n\t" \
    "v_exp_f32 v99, v31\n\t" \
    "s_waitcnt lgkmcnt(2)\n\t" \
    "v_mfma_f32_32x32x16_bf16 v[16:31], v[70:73], v[64:67], v[48:63]\n\t" \
    "ds_read_b128 v[70:73], v100 offset:20992\n\t" \
    "v_cvt_pk_bf16_f32 v90, v96, v97\n\t" \
    "v_cvt_pk_bf16_f32 v91, v98, v99\n\t" \
    "v_exp_f32 v92, v32\n\t" \
    "v_add_u32 v105, 0xa0000, v105\n\t" \
    "v_exp_f32 v93, v33\n\t" \
    "s_add_u32 %[slot], %[slot], %[d1]\n\t" \
    "v_exp_f32 v94, v34\n\t" \
    "v_exp_f32 v95, v35\n\t" \
    "v_mfma_f32_32x32x16_bf16 v[0:15], v[80:83], v[88:91], v[0:15]\n\t" \
    "ds_read_b128 v[80:83], v103 offset:12160\n\t" \
    "s_mov_b64 exec, 0x3ff\n\t" \
    "buffer_load_dword v108, v109, %[w1r], 0 offen\n\t" \
    "buffer_load_dword v108, v110, %[wor], 0 offen\n\t" \
    "s_mov_b64 exec, -1\n\t" \
    "v_cvt_pk_bf16_f32 v84, v92, v93\n\t" \
    "v_cvt_pk_bf16_f32 v85, v94, v95\n\t" \
    "v_exp_f32 v96, v36\n\t" \
    "v_exp_f32 v97, v37\n\t" \
    "v_exp_f32 v98, v38\n\t" \
    "v_exp_f32 v99, v39\n\t" \
    "s_waitcnt lgkmcnt(2)\n\t" \
    "v_mfma_f32_32x32x8_bf16 v[16:31], v[74:75], v[68:69], v[16:31]\n\t" \
    "ds_read_b64 v[74:75], v102 offset:20992\n\t" \
    "v_cvt_pk_bf16_f32 v86, v96, v97\n\t" \
    "v_cvt_pk_bf16_f32 v87, v98, v99\n\t" \
    "v_exp_f32 v92, v40\n\t" \
    "v_add_u32 v109, 0x87000, v109\n\t" \
    "v_exp_f32 v93, v41\n\t" \
    "v_exp_f32 v94, v42\n\t" \
    "v_exp_f32 v95, v43\n\t" \
    "v_mfma_f32_32x32x16_bf16 v[0:15], v[76:79], v[84:87], v[0:15]\n\t" \
    "ds_read_b128 v[76:79], v103 offset:20480\n\t" \
    "v_cvt_pk_bf16_f32 v88, v92, v93\n\t" \
    "v_cvt_pk_bf16_f32 v89, v94, v95\n\t" \
    "v_exp_f32 v96, v44\n\t" \
    "v_add_u32 v110, 0x50000, v110\n\t" \
    "v_exp_f32 v97, v45\n\t" \
    "v_exp_f32 v98, v46\n\t" \
    "v_exp_f32 v99, v47\n\t" \
    "s_waitcnt lgkmcnt(2)\n\t" \
    "v_mfma_f32_32x32x16_bf16 v[32:47], v[70:73], v[64:67], v[48:63]\n\t" \
    "ds_read_b128 v[70:73], v100 offset:30720\n\t" \
    "v_cvt_pk_bf16_f32 v90, v96, v97\n\t" \
    "v_cvt_pk_bf16_f32 v91, v98, v99\n\t" \
    "v_exp_f32 v92, v16\n\t" \
    "v_and_b32 v104, %[d0], v107\n\t" \
    "v_exp_f32 v93, v17\n\t" \
    "v_exp_f32 v94, v18\n\t" \
    "v_exp_f32 v95, v19\n\t" \
    "v_mfma_f32_32x32x16_bf16 v[0:15], v[80:83], v[88:91], v[0:15]\n\t" \
    "ds_read_b128 v[80:83], v103 offset:21120\n\t" \
    "v_cvt_pk_bf16_f32 v84, v92, v93\n\t" \
    "v_cvt_pk_bf16_f32 v85, v94, v95\n\t" \
    "v_exp_f32 v96, v20\n\t" \
    "v_exp_f32 v97, v21\n\t" \
    "v_exp_f32 v98, v22\n\t" \
    "v_exp_f32 v99, v23\n\t" \
    "s_waitcnt lgkmcnt(2)\n\t" \
    "v_mfma_f32_32x32x8_bf16 v[32:47], v[74:75], v[68:69], v[32:47]\n\t" \
    "ds_read_b64 v[74:75], v102 offset:30720\n\t" \
    "v_cvt_pk_bf16_f32 v86, v96, v97\n\t" \
    "v_cvt_pk_bf16_f32 v87, v98, v99\n\t" \
    "v_exp_f32 v92, v24\n\t" \
    "v_exp_f32 v93, v25\n\t" \
    "v_exp_f32 v94, v26\n\t" \
    "v_exp_f32 v95, v27\n\t" \
    "v_mfma_f32_32x32x16_bf16 v[0:15], v[76:79], v[84:87], v[0:15]\n\t" \
    "ds_read_b128 v[76:79], v103 offset:21760\n\t" \
    "v_cvt_pk_bf16_f32 v88, v92, v93\n\t" \
    "v_cvt_pk_bf16_f32 v89, v94, v95\n\t" \
    "v_exp_f32 v96, v28\n\t" \
    "v_exp_f32 v97, v29\n\t" \
    "v_exp_f32 v98, v30\n\t" \
    "v_exp_f32 v99, v31\n\t" \
    "s_waitcnt lgkmcnt(2)\n\t" \
    "v_mfma_f32_32x32x16_bf16 v[16:31], v[70:73], v[64:67], v[48:63]\n\t" \
    "ds_read_b128 v[70:73], v100 offset:31232\n\t" \
    "v_cvt_pk_bf16_f32 v90, v96, v97\n\t" \
    "v_cvt_pk_bf16_f32 v91, v98, v99\n\t" \
    "v_exp_f32 v92, v32\n\t" \
    "v_exp_f32 v93, v33\n\t" \
    "v_exp_f32 v94, v34\n\t" \
    "v_exp_f32 v95, v35\n\t" \
    "v_mfma_f32_32x32x16_bf16 v[0:15], v[80:83], v[88:91], v[0:15]\n\t" \
    "ds_read_b128 v[80:83], v103 offset:22400\n\t" \
    "v_cvt_pk_bf16_f32 v84, v92, v93\n\t" \
    "v_cvt_pk_bf16_f32 v85, v94, v95\n\t" \
    "v_exp_f32 v96, v36\n\t" \
    "v_add_u32 v100, %[d0], v100\n\t" \
    "v_exp_f32 v97, v37\n\t" \
    "v_exp_f32 v98, v38\n\t" \
    "v_exp_f32 v99, v39\n\t" \
    "s_waitcnt lgkmcnt(2)\n\t" \
    "v_mfma_f32_32x32x8_bf16 v[16:31], v[74:75], v[68:69], v[16:31]\n\t" \
    "ds_read_b64 v[74:75], v102 offset:31232\n\t" \
    "v_cvt_pk_bf16_f32 v86, v96, v97\n\t" \
    "v_cvt_pk_bf16_f32 v87, v98, v99\n\t" \
    "v_exp_f32 v92, v40\n\t" \
    "v_exp_f32 v93, v41\n\t" \
    "v_exp_f32 v94, v42\n\t" \
    "v_exp_f32 v95, v43\n\t" \
    "v_mfma_f32_32x32x16_bf16 v[0:15], v[76:79], v[84:87], v[0:15]\n\t" \
    "ds_read_b128 v[76:79], v103 offset:30720\n\t" \
    "v_cvt_pk_bf16_f32 v88, v92, v93\n\t" \
    "v_cvt_pk_bf16_f32 v89, v94, v95\n\t" \
    "v_exp_f32 v96, v44\n\t" \
    "v_add_u32 v102, %[d0], v102\n\t" \
    "v_exp_f32 v97, v45\n\t" \
    "s_mov_b32 %[t0], %[d0]\n\t" \
    "v_exp_f32 v98, v46\n\t" \
    "s_mov_b32 %[d0], %[d1]\n\t" \
    "v_exp_f32 v99, v47\n\t" \
    "s_mov_b32 %[d1], %[d2]\n\t" \
    "s_mov_b32 %[d2], %[t0]\n\t" \
    "s_waitcnt vmcnt(2)\n\t" \
    "s_barrier\n\t" \
    "s_waitcnt lgkmcnt(2)\n\t" \
    "v_mfma_f32_32x32x16_bf16 v[32:47], v[70:73], v[64:67], v[48:63]\n\t" \
    "ds_read_b128 v[70:73], v100 offset:0\n\t" \
    "v_cvt_pk_bf16_f32 v90, v96, v97\n\t" \
    "v_cvt_pk_bf16_f32 v91, v98, v99\n\t" \
    "v_exp_f32 v92, v16\n\t" \
    "v_exp_f32 v93, v17\n\t" \
    "v_exp_f32 v94, v18\n\t" \
    "v_exp_f32 v95, v19\n\t" \
    "v_mfma_f32_32x32x16_bf16 v[0:15], v[80:83], v[88:91], v[0:15]\n\t" \
    "ds_read_b128 v[80:83], v103 offset:31360\n\t" \
    "s_add_u32 m0, %[slot], 2048\n\t" \
    "s_nop 0\n\t" \
    "global_load_lds_dwordx4 v105, %[ws] offset:-2048\n\t" \
    "v_cvt_pk_bf16_f32 v84, v92, v93\n\t" \
    "v_cvt_pk_bf16_f32 v85, v94, v95\n\t" \
    "v_exp_f32 v96, v20\n\t" \
    "v_exp_f32 v97, v21\n\t" \
    "v_exp_f32 v98, v22\n\t" \
    "v_exp_f32 v99, v23\n\t" \
    "s_waitcnt lgkmcnt(2)\n\t" \
    "v_mfma_f32_32x32x8_bf16 v[32:47], v[74:75], v[68:69], v[32:47]\n\t" \
    "ds_read_b64 v[74:75], v102 offset:0\n\t" \
    "v_cvt_pk_bf16_f32 v86, v96, v97\n\t" \
    "v_cvt_pk_bf16_f32 v87, v98, v99\n\t" \
    "v_exp_f32 v92, v24\n\t" \
    "v_exp_f32 v93, v25\n\t" \
    "v_exp_f32 v94, v26\n\t" \
    "v_exp_f32 v95, v27\n\t" \
    "v_mfma_f32_32x32x16_bf16 v[0:15], v[76:79], v[84:87], v[0:15]\n\t" \
    "ds_read_b128 v[76:79], v103 offset:32000\n\t" \
    "v_cvt_pk_bf16_f32 v88, v92, v93\n\t" \
    "v_cvt_pk_bf16_f32 v89, v94, v95\n\t" \
    "v_exp_f32 v96, v28\n\t" \
    "v_exp_f32 v97, v29\n\t" \
    "v_exp_f32 v98, v30\n\t" \
    "v_exp_f32 v99, v31\n\t" \
    "s_waitcnt lgkmcnt(2)\n\t" \
    "v_mfma_f32_32x32x16_bf16 v[16:31], v[70:73], v[64:67], v[48:63]\n\t" \
    "ds_read_b128 v[70:73], v100 offset:512\n\t" \
    "v_cvt_pk_bf16_f32 v90, v96, v97\n\t" \
    "v_cvt_pk_bf16_f32 v91, v98, v99\n\t" \
    "v_exp_f32 v92, v32\n\t" \
    "v_exp_f32 v93, v33\n\t" \
    "v_exp_f32 v94, v34\n\t" \
    "v_exp_f32 v95, v35\n\t" \
    "v_mfma_f32_32x32x16_bf16 v[0:15], v[80:83], v[88:91], v[0:15]\n\t" \
    "ds_read_b128 v[80:83], v103 offset:32640\n\t" \
    "global_load_lds_dwordx4 v105, %[ws] offset:-1024\n\t" \
    "v_cvt_pk_bf16_f32 v84, v92, v93\n\t" \
    "v_cvt_pk_bf16_f32 v85, v94, v95\n\t" \
    "v_exp_f32 v96, v36\n\t" \
    "v_exp_f32 v97, v37\n\t" \
    "v_exp_f32 v98, v38\n\t" \
    "v_exp_f32 v99, v39\n\t" \
    "s_waitcnt lgkmcnt(2)\n\t" \
    "v_mfma_f32_32x32x8_bf16 v[16:31], v[74:75], v[68:69], v[16:31]\n\t" \
    "ds_read_b64 v[74:75], v102 offset:512\n\t" \
    "v_cvt_pk_bf16_f32 v86, v96, v97\n\t" \
    "v_cvt_pk_bf16_f32 v87, v98, v99\n\t" \
    "v_exp_f32 v92, v40\n\t" \
    "v_exp_f32 v93, v41\n\t" \
    "v_exp_f32 v94, v42\n\t" \
    "v_exp_f32 v95, v43\n\t" \
    "v_mfma_f32_32x32x16_bf16 v[0:15], v[76:79], v[84:87], v[0:15]\n\t" \
    "v_add_u32 v103, v104, v103\n\t" \
    "ds_read_b128 v[76:79], v103 offset:0\n\t" \
    "v_cvt_pk_bf16_f32 v88, v92, v93\n\t" \
    "v_cvt_pk_bf16_f32 v89, v94, v95\n\t" \
    "v_exp_f32 v96, v44\n\t" \
    "v_exp_f32 v97, v45\n\t" \
    "v_exp_f32 v98, v46\n\t" \
    "v_exp_f32 v99, v47\n\t" \
    "s_sub_u32 %[n], %[n], 1\n\t" \
    "s_cmp_lg_u32 %[n], 0\n\t" \
    "s_cbranch_scc1 1b\n\t" \
    "s_waitcnt lgkmcnt(2)\n\t" \
    "v_mfma_f32_32x32x16_bf16 v[32:47], v[70:73], v[64:67], v[48:63]\n\t" \
    "ds_read_b128 v[70:73], v100 offset:10240\n\t" \
    "v_cvt_pk_bf16_f32 v90, v96, v97\n\t" \
    "v_cvt_pk_bf16_f32 v91, v98, v99\n\t" \
    "v_exp_f32 v92, v16\n\t" \
    "v_exp_f32 v93, v17\n\t" \
    "v_exp_f32 v94, v18\n\t" \
    "v_exp_f32 v95, v19\n\t" \
    "v_mfma_f32_32x32x16_bf16 v[0:15], v[80:83], v[88:91], v[0:15]\n\t" \
    "ds_read_b128 v[80:83], v103 offset:640\n\t" \
    "global_load_lds_dwordx4 v105, %[ws]\n\t" \
    "v_cvt_pk_bf16_f32 v84, v92, v93\n\t" \
    "v_cvt_pk_bf16_f32 v85, v94, v95\n\t" \
    "v_exp_f32 v96, v20\n\t" \
    "v_exp_f32 v97, v21\n\t" \
    "v_exp_f32 v98, v22\n\t" \
    "v_exp_f32 v99, v23\n\t" \
    "s_waitcnt lgkmcnt(2)\n\t" \
    "v_mfma_f32_32x32x8_bf16 v[32:47], v[74:75], v[68:69], v[32:47]\n\t" \
    "ds_read_b64 v[74:75], v102 offset:10240\n\t" \
    "v_cvt_pk_bf16_f32 v86, v96, v97\n\t" \
    "v_cvt_pk_bf16_f32 v87, v98, v99\n\t" \
    "v_exp_f32 v92, v24\n\t" \
    "v_exp_f32 v93, v25\n\t" \
    "v_exp_f32 v94, v26\n\t" \
    "v_exp_f32 v95, v27\n\t" \
    "v_mfma_f32_32x32x16_bf16 v[0:15], v[76:79], v[84:87], v[0:15]\n\t" \
    "ds_read_b128 v[76:79], v103 offset:1280\n\t" \
    "v_cvt_pk_bf16_f32 v88, v92, v93\n\t" \
    "v_cvt_pk_bf16_f32 v89, v94, v95\n\t" \
    "v_exp_f32 v96, v28\n\t" \
    "v_exp_f32 v97, v29\n\t" \
    "v_exp_f32 v98, v30\n\t" \
    "v_exp_f32 v99, v31\n\t" \
    "s_waitcnt lgkmcnt(2)\n\t" \
    "v_mfma_f32_32x32x16_bf16 v[16:31], v[70:73], v[64:67], v[48:63]\n\t" \
    "ds_read_b128 v[70:73], v100 offset:10752\n\t" \
    "v_cvt_pk_bf16_f32 v90, v96, v97\n\t" \
    "v_cvt_pk_bf16_f32 v91, v98, v99\n\t" \
    "v_exp_f32 v92, v32\n\t" \
    "v_exp_f32 v93, v33\n\t" \
    "v_exp_f32 v94, v34\n\t" \
    "v_exp_f32 v95, v35\n\t" \
    "v_mfma_f32_32x32x16_bf16 v[0:15], v[80:83], v[88:91], v[0:15]\n\t" \
    "ds_read_b128 v[80:83], v103 offset:1920\n\t" \
    "global_load_lds_dwordx4 v105, %[ws] offset:1024\n\t" \
    "v_cvt_pk_bf16_f32 v84, v92, v93\n\t" \
    "v_cvt_pk_bf16_f32 v85, v94, v95\n\t" \
    "v_exp_f32 v96, v36\n\t" \
    "v_exp_f32 v97, v37\n\t" \
    "v_exp_f32 v98, v38\n\t" \
    "v_exp_f32 v99, v39\n\t" \
    "s_waitcnt lgkmcnt(2)\n\t" \
    "v_mfma_f32_32x32x8_bf16 v[16:31], v[74:75], v[68:69], v[16:31]\n\t" \
    "ds_read_b64 v[74:75], v102 offset:10752\n\t" \
    "v_cvt_pk_bf16_f32 v86, v96, v97\n\t" \
    "v_cvt_pk_bf16_f32 v87, v98, v99\n\t" \
    "v_exp_f32 v92, v40\n\t" \
    "v_exp_f32 v93, v41\n\t" \
    "v_exp_f32 v94, v42\n\t" \
    "v_exp_f32 v95, v43\n\t" \
    "v_mfma_f32_32x32x16_bf16 v[0:15], v[76:79], v[84:87], v[0:15]\n\t" \
    "ds_read_b128 v[76:79], v103 offset:10240\n\t" \
    "v_cvt_pk_bf16_f32 v88, v92, v93\n\t" \
    "v_cvt_pk_bf16_f32 v89, v94, v95\n\t" \
    "v_exp_f32 v96, v44\n\t" \
    "v_exp_f32 v97, v45\n\t" \
    "v_exp_f32 v98, v46\n\t" \
    "v_exp_f32 v99, v47\n\t" \
    "s_waitcnt lgkmcnt(2)\n\t" \
    "v_mfma_f32_32x32x16_bf16 v[32:47], v[70:73], v[64:67], v[48:63]\n\t" \
    "ds_read_b128 v[70:73], v100 offset:20480\n\t" \
    "v_cvt_pk_bf16_f32 v90, v96, v97\n\t" \
    "v_cvt_pk_bf16_f32 v91, v98, v99\n\t" \
    "v_exp_f32 v92, v16\n\t" \
    "v_exp_f32 v93, v17\n\t" \
    "v_exp_f32 v94, v18\n\t" \
    "v_exp_f32 v95, v19\n\t" \
    "v_mfma_f32_32x32x16_bf16 v[0:15], v[80:83], v[88:91], v[0:15]\n\t" \
    "ds_read_b128 v[80:83], v103 offset:10880\n\t" \
    "global_load_lds_dwordx4 v105, %[ws] offset:2048\n\t" \
    "v_cvt_pk_bf16_f32 v84, v92, v93\n\t" \
    "v_cvt_pk_bf16_f32 v85, v94, v95\n\t" \
    "v_exp_f32 v96, v20\n\t" \
    "v_exp_f32 v97, v21\n\t" \
    "v_exp_f32 v98, v22\n\t" \
    "v_exp_f32 v99, v23\n\t" \
    "s_waitcnt lgkmcnt(2)\n\t" \
    "v_mfma_f32_32x32x8_bf16 v[32:47], v[74:75], v[68:69], v[32:47]\n\t" \
    "ds_read_b64 v[74:75], v102 offset:20480\n\t" \
    "v_cvt_pk_bf16_f32 v86, v96, v97\n\t" \
    "v_cvt_pk_bf16_f32 v87, v98, v99\n\t" \
    "v_exp_f32 v92, v24\n\t" \
    "v_exp_f32 v93, v25\n\t" \
    "v_exp_f32 v94, v26\n\t" \
    "v_exp_f32 v95, v27\n\t" \
    "v_mfma_f32_32x32x16_bf16 v[0:15], v[76:79], v[84:87], v[0:15]\n\t" \
    "ds_read_b128 v[76:79], v103 offset:11520\n\t" \
    "v_cvt_pk_bf16_f32 v88, v92, v93\n\t" \
    "v_cvt_pk_bf16_f32 v89, v94, v95\n\t" \
    "v_exp_f32 v96, v28\n\t" \
    "v_exp_f32 v97, v29\n\t" \
    "v_exp_f32 v98, v30\n\t" \
    "v_exp_f32 v99, v31\n\t" \
    "s_waitcnt lgkmcnt(2)\n\t" \
    "v_mfma_f32_32x32x16_bf16 v[16:31], v[70:73], v[64:67], v[48:63]\n\t" \
    "ds_read_b128 v[70:73], v100 offset:20992\n\t" \
    "v_cvt_pk_bf16_f32 v90, v96, v97\n\t" \
    "v_cvt_pk_bf16_f32 v91, v98, v99\n\t" \
    "v_exp_f32 v92, v32\n\t" \
    "v_exp_f32 v93, v33\n\t" \
    "v_exp_f32 v94, v34\n\t" \
    "v_exp_f32 v95, v35\n\t" \
    "v_mfma_f32_32x32x16_bf16 v[0:15], v[80:83], v[88:91], v[0:15]\n\t" \
    "ds_read_b128 v[80:83], v103 offset:12160\n\t" \
    "v_cvt_pk_bf16_f32 v84, v92, v93\n\t" \
    "v_cvt_pk_bf16_f32 v85, v94, v95\n\t" \
    "v_exp_f32 v96, v36\n\t" \
    "v_exp_f32 v97, v37\n\t" \
    "v_exp_f32 v98, v38\n\t" \
    "v_exp_f32 v99, v39\n\t" \
    "s_waitcnt lgkmcnt(2)\n\t" \
    "v_mfma_f32_32x32x8_bf16 v[16:31], v[74:75], v[68:69], v[16:31]\n\t" \
    "ds_read_b64 v[74:75], v102 offset:20992\n\t" \
    "v_cvt_pk_bf16_f32 v86, v96, v97\n\t" \
    "v_cvt_pk_bf16_f32 v87, v98, v99\n\t" \
    "v_exp_f32 v92, v40\n\t" \
    "v_exp_f32 v93, v41\n\t" \
    "v_exp_f32 v94, v42\n\t" \
    "v_exp_f32 v95, v43\n\t" \
    "v_mfma_f32_32x32x16_bf16 v[0:15], v[76:79], v[84:87], v[0:15]\n\t" \
    "ds_read_b128 v[76:79], v103 offset:20480\n\t" \
    "v_cvt_pk_bf16_f32 v88, v92, v93\n\t" \
    "v_cvt_pk_bf16_f32 v89, v94, v95\n\t" \
    "v_exp_f32 v96, v44\n\t" \
    "v_exp_f32 v97, v45\n\t" \
    "v_exp_f32 v98, v46\n\t" \
    "v_exp_f32 v99, v47\n\t" \
    "s_waitcnt lgkmcnt(2)\n\t" \
    "v_mfma_f32_32x32x16_bf16 v[32:47], v[70:73], v[64:67], v[48:63]\n\t" \
    "ds_read_b128 v[70:73], v100 offset:30720\n\t" \
    "v_cvt_pk_bf16_f32 v90, v96, v97\n\t" \
    "v_cvt_pk_bf16_f32 v91, v98, v99\n\t" \
    "v_exp_f32 v92, v16\n\t" \
    "v_and_b32 v104, %[d0], v107\n\t" \
    "v_exp_f32 v93, v17\n\t" \
    "v_exp_f32 v94, v18\n\t" \
    "v_exp_f32 v95, v19\n\t" \
    "v_mfma_f32_32x32x16_bf16 v[0:15], v[80:83], v[88:91], v[0:15]\n\t" \
    "ds_read_b128 v[80:83], v103 offset:21120\n\t" \
    "v_cvt_pk_bf16_f32 v84, v92, v93\n\t" \
    "v_cvt_pk_bf16_f32 v85, v94, v95\n\t" \
    "v_exp_f32 v96, v20\n\t" \
    "v_exp_f32 v97, v21\n\t" \
    "v_exp_f32 v98, v22\n\t" \
    "v_exp_f32 v99, v23\n\t" \
    "s_waitcnt lgkmcnt(2)\n\t" \
    "v_mfma_f32_32x32x8_bf16 v[32:47], v[74:75], v[68:69], v[32:47]\n\t" \
    "ds_read_b64 v[74:75], v102 offset:30720\n\t" \
    "v_cvt_pk_bf16_f32 v86, v96, v97\n\t" \
    "v_cvt_pk_bf16_f32 v87, v98, v99\n\t" \
    "v_exp_f32 v92, v24\n\t" \
    "v_exp_f32 v93, v25\n\t" \
    "v_exp_f32 v94, v26\n\t" \
    "v_exp_f32 v95, v27\n\t" \
    "v_mfma_f32_32x32x16_bf16 v[0:15], v[76:79], v[84:87], v[0:15]\n\t" \
    "ds_read_b128 v[76:79], v103 offset:21760\n\t" \
    "v_cvt_pk_bf16_f32 v88, v92, v93\n\t" \
    "v_cvt_pk_bf16_f32 v89, v94, v95\n\t" \
    "v_exp_f32 v96, v28\n\t" \
    "v_exp_f32 v97, v29\n\t" \
    "v_exp_f32 v98, v30\n\t" \
    "v_exp_f32 v99, v31\n\t" \
    "s_waitcnt lgkmcnt(2)\n\t" \
    "v_mfma_f32_32x32x16_bf16 v[16:31], v[70:73], v[64:67], v[48:63]\n\t" \
    "ds_read_b128 v[70:73], v100 offset:31232\n\t" \
    "v_cvt_pk_bf16_f32 v90, v96, v97\n\t" \
    "v_cvt_pk_bf16_f32 v91, v98, v99\n\t" \
    "v_exp_f32 v92, v32\n\t" \
    "v_exp_f32 v93, v33\n\t" \
    "v_exp_f32 v94, v34\n\t" \
    "v_exp_f32 v95, v35\n\t" \
    "v_mfma_f32_32x32x16_bf16 v[0:15], v[80:83], v[88:91], v[0:15]\n\t" \
    "ds_read_b128 v[80:83], v103 offset:22400\n\t" \
    "v_cvt_pk_bf16_f32 v84, v92, v93\n\t" \
    "v_cvt_pk_bf16_f32 v85, v94, v95\n\t" \
    "v_exp_f32 v96, v36\n\t" \
    "v_add_u32 v100, %[d0], v100\n\t" \
    "v_exp_f32 v97, v37\n\t" \
    "v_exp_f32 v98, v38\n\t" \
    "v_exp_f32 v99, v39\n\t" \
    "s_waitcnt lgkmcnt(2)\n\t" \
    "v_mfma_f32_32x32x8_bf16 v[16:31], v[74:75], v[68:69], v[16:31]\n\t" \
    "ds_read_b64 v[74:75], v102 offset:31232\n\t" \
    "v_cvt_pk_bf16_f32 v86, v96, v97\n\t" \
    "v_cvt_pk_bf16_f32 v87, v98, v99\n\t" \
    "v_exp_f32 v92, v40\n\t" \
    "v_exp_f32 v93, v41\n\t" \
    "v_exp_f32 v94, v42\n\t" \
    "v_exp_f32 v95, v43\n\t" \
    "v_mfma_f32_32x32x16_bf16 v[0:15], v[76:79], v[84:87], v[0:15]\n\t" \
    "ds_read_b128 v[76:79], v103 offset:30720\n\t" \
    "v_cvt_pk_bf16_f32 v88, v92, v93\n\t" \
    "v_cvt_pk_bf16_f32 v89, v94, v95\n\t" \
    "v_exp_f32 v96, v44\n\t" \
    "v_add_u32 v102, %[d0], v102\n\t" \
    "v_exp_f32 v97, v45\n\t" \
    "s_mov_b32 %[t0], %[d0]\n\t" \
    "v_exp_f32 v98, v46\n\t" \
    "s_mov_b32 %[d0], %[d1]\n\t" \
    "v_exp_f32 v99, v47\n\t" \
    "s_mov_b32 %[d1], %[d2]\n\t" \
    "s_mov_b32 %[d2], %[t0]\n\t" \
    "s_waitcnt vmcnt(0)\n\t" \
    "s_barrier\n\t" \
    "s_waitcnt lgkmcnt(2)\n\t" \
    "v_mfma_f32_32x32x16_bf16 v[32:47], v[70:73], v[64:67], v[48:63]\n\t" \
    "ds_read_b128 v[70:73], v100 offset:0\n\t" \
    "v_cvt_pk_bf16_f32 v90, v96, v97\n\t" \
    "v_cvt_pk_bf16_f32 v91, v98, v99\n\t" \
    "v_exp_f32 v92, v16\n\t" \
    "v_exp_f32 v93, v17\n\t" \
    "v_exp_f32 v94, v18\n\t" \
    "v_exp_f32 v95, v19\n\t" \
    "v_mfma_f32_32x32x16_bf16 v[0:15], v[80:83], v[88:91], v[0:15]\n\t" \
    "ds_read_b128 v[80:83], v103 offset:31360\n\t" \
    "v_cvt_pk_bf16_f32 v84, v92, v93\n\t" \
    "v_cvt_pk_bf16_f32 v85, v94, v95\n\t" \
    "v_exp_f32 v96, v20\n\t" \
    "v_exp_f32 v97, v21\n\t" \
    "v_exp_f32 v98, v22\n\t" \
    "v_exp_f32 v99, v23\n\t" \
    "s_waitcnt lgkmcnt(2)\n\t" \
    "v_mfma_f32_32x32x8_bf16 v[32:47], v[74:75], v[68:69], v[32:47]\n\t" \
    "ds_read_b64 v[74:75], v102 offset:0\n\t" \
    "v_cvt_pk_bf16_f32 v86, v96, v97\n\t" \
    "v_cvt_pk_bf16_f32 v87, v98, v99\n\t" \
    "v_exp_f32 v92, v24\n\t" \
    "v_exp_f32 v93, v25\n\t" \
    "v_exp_f32 v94, v26\n\t" \
    "v_exp_f32 v95, v27\n\t" \
    "v_mfma_f32_32x32x16_bf16 v[0:15], v[76:79], v[84:87], v[0:15]\n\t" \
    "ds_read_b128 v[76:79], v103 offset:32000\n\t" \
    "v_cvt_pk_bf16_f32 v88, v92, v93\n\t" \
    "v_cvt_pk_bf16_f32 v89, v94, v95\n\t" \
    "v_exp_f32 v96, v28\n\t" \
    "v_exp_f32 v97, v29\n\t" \
    "v_exp_f32 v98, v30\n\t" \
    "v_exp_f32 v99, v31\n\t" \
    "s_waitcnt lgkmcnt(2)\n\t" \
    "v_mfma_f32_32x32x16_bf16 v[16:31], v[70:73], v[64:67], v[48:63]\n\t" \
    "ds_read_b128 v[70:73], v100 offset:512\n\t" \
    "v_cvt_pk_bf16_f32 v90, v96, v97\n\t" \
    "v_cvt_pk_bf16_f32 v91, v98, v99\n\t" \
    "v_exp_f32 v92, v32\n\t" \
    "v_exp_f32 v93, v33\n\t" \
    "v_exp_f32 v94, v34\n\t" \
    "v_exp_f32 v95, v35\n\t" \
    "v_mfma_f32_32x32x16_bf16 v[0:15], v[80:83], v[88:91], v[0:15]\n\t" \
    "ds_read_b128 v[80:83], v103 offset:32640\n\t" \
    "v_cvt_pk_bf16_f32 v84, v92, v93\n\t" \
    "v_cvt_pk_bf16_f32 v85, v94, v95\n\t" \
    "v_exp_f32 v96, v36\n\t" \
    "v_exp_f32 v97, v37\n\t" \
    "v_exp_f32 v98, v38\n\t" \
    "v_exp_f32 v99, v39\n\t" \
    "s_waitcnt lgkmcnt(2)\n\t" \
    "v_mfma_f32_32x32x8_bf16 v[16:31], v[74:75], v[68:69], v[16:31]\n\t" \
    "ds_read_b64 v[74:75], v102 offset:512\n\t" \
    "v_cvt_pk_bf16_f32 v86, v96, v97\n\t" \
    "v_cvt_pk_bf16_f32 v87, v98, v99\n\t" \
    "v_exp_f32 v92, v40\n\t" \
    "v_exp_f32 v93, v41\n\t" \
    "v_exp_f32 v94, v42\n\t" \
    "v_exp_f32 v95, v43\n\t" \
    "v_mfma_f32_32x32x16_bf16 v[0:15], v[76:79], v[84:87], v[0:15]\n\t" \
    "v_add_u32 v103, v104, v103\n\t" \
    "ds_read_b128 v[76:79], v103 offset:0\n\t" \
    "v_cvt_pk_bf16_f32 v88, v92, v93\n\t" \
    "v_cvt_pk_bf16_f32 v89, v94, v95\n\t" \
    "v_exp_f32 v96, v44\n\t" \
    "v_exp_f32 v97, v45\n\t" \
    "v_exp_f32 v98, v46\n\t" \
    "v_exp_f32 v99, v47\n\t" \
    "s_waitcnt lgkmcnt(2)\n\t" \
    "v_mfma_f32_32x32x16_bf16 v[32:47], v[70:73], v[64:67], v[48:63]\n\t" \
    "ds_read_b128 v[70:73], v100 offset:10240\n\t" \
    "v_cvt_pk_bf16_f32 v90, v96, v97\n\t" \
    "v_cvt_pk_bf16_f32 v91, v98, v99\n\t" \
    "v_exp_f32 v92, v16\n\t" \
    "v_exp_f32 v93, v17\n\t" \
    "v_exp_f32 v94, v18\n\t" \
    "v_exp_f32 v95, v19\n\t" \
    "v_mfma_f32_32x32x16_bf16 v[0:15], v[80:83], v[88:91], v[0:15]\n\t" \
    "ds_read_b128 v[80:83], v103 offset:640\n\t" \
    "v_cvt_pk_bf16_f32 v84, v92, v93\n\t" \
    "v_cvt_pk_bf16_f32 v85, v94, v95\n\t" \
    "v_exp_f32 v96, v20\n\t" \
    "v_exp_f32 v97, v21\n\t" \
    "v_exp_f32 v98, v22\n\t" \
    "v_exp_f32 v99, v23\n\t" \
    "s_waitcnt lgkmcnt(2)\n\t" \
    "v_mfma_f32_32x32x8_bf16 v[32:47], v[74:75], v[68:69], v[32:47]\n\t" \
    "ds_read_b64 v[74:75], v102 offset:10240\n\t" \
    "v_cvt_pk_bf16_f32 v86, v96, v97\n\t" \
    "v_cvt_pk_bf16_f32 v87, v98, v99\n\t" \
    "v_exp_f32 v92, v24\n\t" \
    "v_exp_f32 v93, v25\n\t" \
    "v_exp_f32 v94, v26\n\t" \
    "v_exp_f32 v95, v27\n\t" \
    "v_mfma_f32_32x32x16_bf16 v[0:15], v[76:79], v[84:87], v[0:15]\n\t" \
    "ds_read_b128 v[76:79], v103 offset:1280\n\t" \
    "v_cvt_pk_bf16_f32 v88, v92, v93\n\t" \
    "v_cvt_pk_bf16_f32 v89, v94, v95\n\t" \
    "v_exp_f32 v96, v28\n\t" \
    "v_exp_f32 v97, v29\n\t" \
    "v_exp_f32 v98, v30\n\t" \
    "v_exp_f32 v99, v31\n\t" \
    "s_waitcnt lgkmcnt(2)\n\t" \
    "v_mfma_f32_32x32x16_bf16 v[16:31], v[70:73], v[64:67], v[48:63]\n\t" \
    "ds_read_b128 v[70:73], v100 offset:10752\n\t" \
    "v_cvt_pk_bf16_f32 v90, v96, v97\n\t" \
    "v_cvt_pk_bf16_f32 v91, v98, v99\n\t" \
    "v_exp_f32 v92, v32\n\t" \
    "v_exp_f32 v93, v33\n\t" \
    "v_exp_f32 v94, v34\n\t" \
    "v_exp_f32 v95, v35\n\t" \
    "v_mfma_f32_32x32x16_bf16 v[0:15], v[80:83], v[88:91], v[0:15]\n\t" \
    "ds_read_b128 v[80:83], v103 offset:1920\n\t" \
    "v_cvt_pk_bf16_f32 v84, v92, v93\n\t" \
    "v_cvt_pk_bf16_f32 v85, v94, v95\n\t" \
    "v_exp_f32 v96, v36\n\t" \
    "v_exp_f32 v97, v37\n\t" \
    "v_exp_f32 v98, v38\n\t" \
    "v_exp_f32 v99, v39\n\t" \
    "s_waitcnt lgkmcnt(2)\n\t" \
    "v_mfma_f32_32x32x8_bf16 v[16:31], v[74:75], v[68:69], v[16:31]\n\t" \
    "ds_read_b64 v[74:75], v102 offset:10752\n\t" \
    "v_cvt_pk_bf16_f32 v86, v96, v97\n\t" \
    "v_cvt_pk_bf16_f32 v87, v98, v99\n\t" \
    "v_exp_f32 v92, v40\n\t" \
    "v_exp_f32 v93, v41\n\t" \
    "v_exp_f32 v94, v42\n\t" \
    "v_exp_f32 v95, v43\n\t" \
    "v_mfma_f32_32x32x16_bf16 v[0:15], v[76:79], v[84:87], v[0:15]\n\t" \
    "ds_read_b128 v[76:79], v103 offset:10240\n\t" \
    "v_cvt_pk_bf16_f32 v88, v92, v93\n\t" \
    "v_cvt_pk_bf16_f32 v89, v94, v95\n\t" \
    "v_exp_f32 v96, v44\n\t" \
    "v_exp_f32 v97, v45\n\t" \
    "v_exp_f32 v98, v46\n\t" \
    "v_exp_f32 v99, v47\n\t" \
    "s_waitcnt lgkmcnt(2)\n\t" \
    "v_mfma_f32_32x32x16_bf16 v[32:47], v[70:73], v[64:67], v[48:63]\n\t" \
    "ds_read_b128 v[70:73], v100 offset:20480\n\t" \
    "v_cvt_pk_bf16_f32 v90, v96, v97\n\t" \
    "v_cvt_pk_bf16_f32 v91, v98, v99\n\t" \
    "v_exp_f32 v92, v16\n\t" \
    "v_exp_f32 v93, v17\n\t" \
    "v_exp_f32 v94, v18\n\t" \
    "v_exp_f32 v95, v19\n\t" \
    "v_mfma_f32_32x32x16_bf16 v[0:15], v[80:83], v[88:91], v[0:15]\n\t" \
    "ds_read_b128 v[80:83], v103 offset:10880\n\t" \
    "v_cvt_pk_bf16_f32 v84, v92, v93\n\t" \
    "v_cvt_pk_bf16_f32 v85, v94, v95\n\t" \
    "v_exp_f32 v96, v20\n\t" \
    "v_exp_f32 v97, v21\n\t" \
    "v_exp_f32 v98, v22\n\t" \
    "v_exp_f32 v99, v23\n\t" \
    "s_waitcnt lgkmcnt(2)\n\t" \
    "v_mfma_f32_32x32x8_bf16 v[32:47], v[74:75], v[68:69], v[32:47]\n\t" \
    "ds_read_b64 v[74:75], v102 offset:20480\n\t" \
    "v_cvt_pk_bf16_f32 v86, v96, v97\n\t" \
    "v_cvt_pk_bf16_f32 v87, v98, v99\n\t" \
    "v_exp_f32 v92, v24\n\t" \
    "v_exp_f32 v93, v25\n\t" \
    "v_exp_f32 v94, v26\n\t" \
    "v_exp_f32 v95, v27\n\t" \
    "v_mfma_f32_32x32x16_bf16 v[0:15], v[76:79], v[84:87], v[0:15]\n\t" \
    "ds_read_b128 v[76:79], v103 offset:11520\n\t" \
    "v_cvt_pk_bf16_f32 v88, v92, v93\n\t" \
    "v_cvt_pk_bf16_f32 v89, v94, v95\n\t" \
    "v_exp_f32 v96, v28\n\t" \
    "v_exp_f32 v97, v29\n\t" \
    "v_exp_f32 v98, v30\n\t" \
    "v_exp_f32 v99, v31\n\t" \
    "s_waitcnt lgkmcnt(2)\n\t" \
    "v_mfma_f32_32x32x16_bf16 v[16:31], v[70:73], v[64:67], v[48:63]\n\t" \
    "ds_read_b128 v[70:73], v100 offset:20992\n\t" \
    "v_cvt_pk_bf16_f32 v90, v96, v97\n\t" \
    "v_cvt_pk_bf16_f32 v91, v98, v99\n\t" \
    "v_exp_f32 v92, v32\n\t" \
    "v_exp_f32 v93, v33\n\t" \
    "v_exp_f32 v94, v34\n\t" \
    "v_exp_f32 v95, v35\n\t" \
    "v_mfma_f32_32x32x16_bf16 v[0:15], v[80:83], v[88:91], v[0:15]\n\t" \
    "ds_read_b128 v[80:83], v103 offset:12160\n\t" \
    "v_cvt_pk_bf16_f32 v84, v92, v93\n\t" \
    "v_cvt_pk_bf16_f32 v85, v94, v95\n\t" \
    "v_exp_f32 v96, v36\n\t" \
    "v_exp_f32 v97, v37\n\t" \
    "v_exp_f32 v98, v38\n\t" \
    "v_exp_f32 v99, v39\n\t" \
    "s_waitcnt lgkmcnt(2)\n\t" \
    "v_mfma_f32_32x32x8_bf16 v[16:31], v[74:75], v[68:69], v[16:31]\n\t" \
    "ds_read_b64 v[74:75], v102 offset:20992\n\t" \
    "v_cvt_pk_bf16_f32 v86, v96, v97\n\t" \
    "v_cvt_pk_bf16_f32 v87, v98, v99\n\t" \
    "v_exp_f32 v92, v40\n\t" \
    "v_exp_f32 v93, v41\n\t" \
    "v_exp_f32 v94, v42\n\t" \
    "v_exp_f32 v95, v43\n\t" \
    "v_mfma_f32_32x32x16_bf16 v[0:15], v[76:79], v[84:87], v[0:15]\n\t" \
    "ds_read_b128 v[76:79], v103 offset:20480\n\t" \
    "v_cvt_pk_bf16_f32 v88, v92, v93\n\t" \
    "v_cvt_pk_bf16_f32 v89, v94, v95\n\t" \
    "v_exp_f32 v96, v44\n\t" \
    "v_exp_f32 v97, v45\n\t" \
    "v_exp_f32 v98, v46\n\t" \
    "v_exp_f32 v99, v47\n\t" \
    "s_waitcnt lgkmcnt(2)\n\t" \
    "v_mfma_f32_32x32x16_bf16 v[32:47], v[70:73], v[64:67], v[48:63]\n\t" \
    "ds_read_b128 v[70:73], v100 offset:30720\n\t" \
    "v_cvt_pk_bf16_f32 v90, v96, v97\n\t" \
    "v_cvt_pk_bf16_f32 v91, v98, v99\n\t" \
    "v_exp_f32 v92, v16\n\t" \
    "v_exp_f32 v93, v17\n\t" \
    "v_exp_f32 v94, v18\n\t" \
    "v_exp_f32 v95, v19\n\t" \
    "v_mfma_f32_32x32x16_bf16 v[0:15], v[80:83], v[88:91], v[0:15]\n\t" \
    "ds_read_b128 v[80:83], v103 offset:21120\n\t" \
    "v_cvt_pk_bf16_f32 v84, v92, v93\n\t" \
    "v_cvt_pk_bf16_f32 v85, v94, v95\n\t" \
    "v_exp_f32 v96, v20\n\t" \
    "v_exp_f32 v97, v21\n\t" \
    "v_exp_f32 v98, v22\n\t" \
    "v_exp_f32 v99, v23\n\t" \
    "s_waitcnt lgkmcnt(2)\n\t" \
    "v_mfma_f32_32x32x8_bf16 v[32:47], v[74:75], v[68:69], v[32:47]\n\t" \
    "ds_read_b64 v[74:75], v102 offset:30720\n\t" \
    "v_cvt_pk_bf16_f32 v86, v96, v97\n\t" \
    "v_cvt_pk_bf16_f32 v87, v98, v99\n\t" \
    "v_exp_f32 v92, v24\n\t" \
    "v_exp_f32 v93, v25\n\t" \
    "v_exp_f32 v94, v26\n\t" \
    "v_exp_f32 v95, v27\n\t" \
    "v_mfma_f32_32x32x16_bf16 v[0:15], v[76:79], v[84:87], v[0:15]\n\t" \
    "ds_read_b128 v[76:79], v103 offset:21760\n\t" \
    "v_cvt_pk_bf16_f32 v88, v92, v93\n\t" \
    "v_cvt_pk_bf16_f32 v89, v94, v95\n\t" \
    "v_exp_f32 v96, v28\n\t" \
    "v_exp_f32 v97, v29\n\t" \
    "v_exp_f32 v98, v30\n\t" \
    "v_exp_f32 v99, v31\n\t" \
    "s_waitcnt lgkmcnt(2)\n\t" \
    "v_mfma_f32_32x32x16_bf16 v[16:31], v[70:73], v[64:67], v[48:63]\n\t" \
    "ds_read_b128 v[70:73], v100 offset:31232\n\t" \
    "v_cvt_pk_bf16_f32 v90, v96, v97\n\t" \
    "v_cvt_pk_bf16_f32 v91, v98, v99\n\t" \
    "v_exp_f32 v92, v32\n\t" \
    "v_exp_f32 v93, v33\n\t" \
    "v_exp_f32 v94, v34\n\t" \
    "v_exp_f32 v95, v35\n\t" \
    "v_mfma_f32_32x32x16_bf16 v[0:15], v[80:83], v[88:91], v[0:15]\n\t" \
    "ds_read_b128 v[80:83], v103 offset:22400\n\t" \
    "v_cvt_pk_bf16_f32 v84, v92, v93\n\t" \
    "v_cvt_pk_bf16_f32 v85, v94, v95\n\t" \
    "v_exp_f32 v96, v36\n\t" \
    "v_exp_f32 v97, v37\n\t" \
    "v_exp_f32 v98, v38\n\t" \
    "v_exp_f32 v99, v39\n\t" \
    "s_waitcnt lgkmcnt(2)\n\t" \
    "v_mfma_f32_32x32x8_bf16 v[16:31], v[74:75], v[68:69], v[16:31]\n\t" \
    "ds_read_b64 v[74:75], v102 offset:31232\n\t" \
    "v_cvt_pk_bf16_f32 v86, v96, v97\n\t" \
    "v_cvt_pk_bf16_f32 v87, v98, v99\n\t" \
    "v_exp_f32 v92, v40\n\t" \
    "v_exp_f32 v93, v41\n\t" \
    "v_exp_f32 v94, v42\n\t" \
    "v_exp_f32 v95, v43\n\t" \
    "v_mfma_f32_32x32x16_bf16 v[0:15], v[76:79], v[84:87], v[0:15]\n\t" \
    "ds_read_b128 v[76:79], v103 offset:30720\n\t" \
    "v_cvt_pk_bf16_f32 v88, v92, v93\n\t" \
    "v_cvt_pk_bf16_f32 v89, v94, v95\n\t" \
    "v_exp_f32 v96, v44\n\t" \
    "v_exp_f32 v97, v45\n\t" \
    "v_exp_f32 v98, v46\n\t" \
    "v_exp_f32 v99, v47\n\t" \
    "s_waitcnt lgkmcnt(2)\n\t" \
    "v_mfma_f32_32x32x16_bf16 v[32:47], v[70:73], v[64:67], v[48:63]\n\t" \
    "ds_read_b128 v[70:73], v100 offset:0\n\t" \
    "v_cvt_pk_bf16_f32 v90, v96, v97\n\t" \
    "v_cvt_pk_bf16_f32 v91, v98, v99\n\t" \
    "v_exp_f32 v92, v16\n\t" \
    "v_exp_f32 v93, v17\n\t" \
    "v_exp_f32 v94, v18\n\t" \
    "v_exp_f32 v95, v19\n\t" \
    "v_mfma_f32_32x32x16_bf16 v[0:15], v[80:83], v[88:91], v[0:15]\n\t" \
    "ds_read_b128 v[80:83], v103 offset:31360\n\t" \
    "v_cvt_pk_bf16_f32 v84, v92, v93\n\t" \
    "v_cvt_pk_bf16_f32 v85, v94, v95\n\t" \
    "v_exp_f32 v96, v20\n\t" \
    "v_exp_f32 v97, v21\n\t" \
    "v_exp_f32 v98, v22\n\t" \
    "v_exp_f32 v99, v23\n\t" \
    "s_waitcnt lgkmcnt(2)\n\t" \
    "v_mfma_f32_32x32x8_bf16 v[32:47], v[74:75], v[68:69], v[32:47]\n\t" \
    "ds_read_b64 v[74:75], v102 offset:0\n\t" \
    "v_cvt_pk_bf16_f32 v86, v96, v97\n\t" \
    "v_cvt_pk_bf16_f32 v87, v98, v99\n\t" \
    "v_exp_f32 v92, v24\n\t" \
    "v_exp_f32 v93, v25\n\t" \
    "v_exp_f32 v94, v26\n\t" \
    "v_exp_f32 v95, v27\n\t" \
    "v_mfma_f32_32x32x16_bf16 v[0:15], v[76:79], v[84:87], v[0:15]\n\t" \
    "ds_read_b128 v[76:79], v103 offset:32000\n\t" \
    "v_cvt_pk_bf16_f32 v88, v92, v93\n\t" \
    "v_cvt_pk_bf16_f32 v89, v94, v95\n\t" \
    "v_exp_f32 v96, v28\n\t" \
    "v_exp_f32 v97, v29\n\t" \
    "v_exp_f32 v98, v30\n\t" \
    "v_exp_f32 v99, v31\n\t" \
    "s_waitcnt lgkmcnt(2)\n\t" \
    "v_mfma_f32_32x32x16_bf16 v[16:31], v[70:73], v[64:67], v[48:63]\n\t" \
    "ds_read_b128 v[70:73], v100 offset:512\n\t" \
    "v_cvt_pk_bf16_f32 v90, v96, v97\n\t" \
    "v_cvt_pk_bf16_f32 v91, v98, v99\n\t" \
    "v_exp_f32 v92, v32\n\t" \
    "v_exp_f32 v93, v33\n\t" \
    "v_exp_f32 v94, v34\n\t" \
    "v_exp_f32 v95, v35\n\t" \
    "v_mfma_f32_32x32x16_bf16 v[0:15], v[80:83], v[88:91], v[0:15]\n\t" \
    "ds_read_b128 v[80:83], v103 offset:32640\n\t" \
    "v_cvt_pk_bf16_f32 v84, v92, v93\n\t" \
    "v_cvt_pk_bf16_f32 v85, v94, v95\n\t" \
    "v_exp_f32 v96, v36\n\t" \
    "v_exp_f32 v97, v37\n\t" \
    "v_exp_f32 v98, v38\n\t" \
    "v_exp_f32 v99, v39\n\t" \
    "s_waitcnt lgkmcnt(2)\n\t" \
    "v_mfma_f32_32x32x8_bf16 v[16:31], v[74:75], v[68:69], v[16:31]\n\t" \
    "ds_read_b64 v[74:75], v102 offset:512\n\t" \
    "v_cvt_pk_bf16_f32 v86, v96, v97\n\t" \
    "v_cvt_pk_bf16_f32 v87, v98, v99\n\t" \
    "v_exp_f32 v92, v40\n\t" \
    "v_exp_f32 v93, v41\n\t" \
    "v_exp_f32 v94, v42\n\t" \
    "v_exp_f32 v95, v43\n\t" \
    "v_mfma_f32_32x32x16_bf16 v[0:15], v[76:79], v[84:87], v[0:15]\n\t" \
    "ds_read_b128 v[76:79], v103 offset:0\n\t" \
    "v_cvt_pk_bf16_f32 v88, v92, v93\n\t" \
    "v_cvt_pk_bf16_f32 v89, v94, v95\n\t" \
    "v_exp_f32 v96, v44\n\t" \
    "v_exp_f32 v97, v45\n\t" \
    "v_exp_f32 v98, v46\n\t" \
    "v_exp_f32 v99, v47\n\t" \
    "s_waitcnt lgkmcnt(0)\n\t"
#define G_LOOP_QK \
    "s_barrier\n\t" \
    "ds_read_b128 v[64:67], v112\n\t" \
    "ds_read_b64_tr_b16 v[72:73], v120\n\t" \
    "ds_read_b64_tr_b16 v[74:75], v120 offset:1024\n\t" \
    "ds_read_b64_tr_b16 v[76:77], v121\n\t" \
    "ds_read_b64_tr_b16 v[78:79], v121 offset:1024\n\t" \
    "ds_read_b128 v[68:71], v116\n\t" \
    "s_mov_b32 %0, 5\n\t" \
    "1:\n\t" \
    "s_waitcnt lgkmcnt(3)\n\t" \
    "v_mfma_f32_32x32x16_bf16 v[0:15], v[72:75], v[64:67], v[0:15]\n\t" \
    "ds_read_b128 v[80:83], v113 offset:0\n\t" \
    "ds_read_b64_tr_b16 v[88:89], v120 offset:4096\n\t" \
    "v_lshlrev_b32 v106, 16, v64\n\t" \
    "v_and_b32 v107, 0xffff0000, v64\n\t" \
    "v_fmac_f32 v104, v106, v106\n\t" \
    "v_fmac_f32 v105, v107, v107\n\t" \
    "s_waitcnt lgkmcnt(3)\n\t" \
    "v_mfma_f32_32x32x16_bf16 v[16:31], v[76:79], v[64:67], v[16:31]\n\t" \
    "ds_read_b64_tr_b16 v[90:91], v120 offset:5120\n\t" \
    "ds_read_b64_tr_b16 v[92:93], v121 offset:4096\n\t" \
    "v_lshlrev_b32 v106, 16, v65\n\t" \
    "v_and_b32 v107, 0xffff0000, v65\n\t" \
    "v_fmac_f32 v104, v106, v106\n\t" \
    "v_fmac_f32 v105, v107, v107\n\t" \
    "s_waitcnt lgkmcnt(4)\n\t" \
    "v_mfma_f32_32x32x16_bf16 v[32:47], v[72:75], v[68:71], v[32:47]\n\t" \
    "ds_read_b64_tr_b16 v[94:95], v121 offset:5120\n\t" \
    "ds_read_b128 v[84:87], v117 offset:0\n\t" \
    "v_lshlrev_b32 v106, 16, v66\n\t" \
    "v_and_b32 v107, 0xffff0000, v66\n\t" \
    "v_fmac_f32 v104, v106, v106\n\t" \
    "v_fmac_f32 v105, v107, v107\n\t" \
    "v_mfma_f32_16x16x32_bf16 v[96:99], v[108:111], v[72:75], v[96:99]\n\t" \
    "v_mfma_f32_32x32x16_bf16 v[48:63], v[76:79], v[68:71], v[48:63]\n\t" \
    "v_lshlrev_b32 v106, 16, v67\n\t" \
    "v_and_b32 v107, 0xffff0000, v67\n\t" \
    "v_fmac_f32 v104, v106, v106\n\t" \
    "v_fmac_f32 v105, v107, v107\n\t" \
    "v_mfma_f32_16x16x32_bf16 v[100:103], v[108:111], v[64:67], v[100:103]\n\t" \
    "s_waitcnt lgkmcnt(3)\n\t" \
    "v_mfma_f32_32x32x16_bf16 v[0:15], v[88:91], v[80:83], v[0:15]\n\t" \
    "ds_read_b128 v[64:67], v114 offset:0\n\t" \
    "ds_read_b64_tr_b16 v[72:73], v120 offset:8192\n\t" \
    "v_lshlrev_b32 v106, 16, v80\n\t" \
    "v_and_b32 v107, 0xffff0000, v80\n\t" \
    "v_fmac_f32 v104, v106, v106\n\t" \
    "v_fmac_f32 v105, v107, v107\n\t" \
    "s_waitcnt lgkmcnt(3)\n\t" \
    "v_mfma_f32_32x32x16_bf16 v[16:31], v[92:95], v[80:83], v[16:31]\n\t" \
    "ds_read_b64_tr_b16 v[74:75], v120 offset:9216\n\t" \
    "ds_read_b64_tr_b16 v[76:77], v121 offset:8192\n\t" \
    "v_lshlrev_b32 v106, 16, v81\n\t" \
    "v_and_b32 v107, 0xffff0000, v81\n\t" \
    "v_fmac_f32 v104, v106, v106\n\t" \
    "v_fmac_f32 v105, v107, v107\n\t" \
    "s_waitcnt lgkmcnt(4)\n\t" \
    "v_mfma_f32_32x32x16_bf16 v[32:47], v[88:91], v[84:87], v[32:47]\n\t" \
    "ds_read_b64_tr_b16 v[78:79], v121 offset:9216\n\t" \
    "ds_read_b128 v[68:71], v118 offset:0\n\t" \
    "v_lshlrev_b32 v106, 16, v82\n\t" \
    "v_and_b32 v107, 0xffff0000, v82\n\t" \
    "v_fmac_f32 v104, v106, v106\n\t" \
    "v_fmac_f32 v105, v107, v107\n\t" \
    "v_mfma_f32_16x16x32_bf16 v[96:99], v[108:111], v[88:91], v[96:99]\n\t" \
    "v_mfma_f32_32x32x16_bf16 v[48:63], v[92:95], v[84:87], v[48:63]\n\t" \
    "v_lshlrev_b32 v106, 16, v83\n\t" \
    "v_and_b32 v107, 0xffff0000, v83\n\t" \
    "v_fmac_f32 v104, v106, v106\n\t" \
    "v_fmac_f32 v105, v107, v107\n\t" \
    "v_mfma_f32_16x16x32_bf16 v[100:103], v[108:111], v[80:83], v[100:103]\n\t" \
    "s_waitcnt lgkmcnt(3)\n\t" \
    "v_mfma_f32_32x32x16_bf16 v[0:15], v[72:75], v[64:67], v[0:15]\n\t" \
    "ds_read_b128 v[80:83], v115 offset:0\n\t" \
    "ds_read_b64_tr_b16 v[88:89], v120 offset:12288\n\t" \
    "v_lshlrev_b32 v106, 16, v64\n\t" \
    "v_and_b32 v107, 0xffff0000, v64\n\t" \
    "v_fmac_f32 v104, v106, v106\n\t" \
    "v_fmac_f32 v105, v107, v107\n\t" \
    "s_waitcnt lgkmcnt(3)\n\t" \
    "v_mfma_f32_32x32x16_bf16 v[16:31], v[76:79], v[64:67], v[16:31]\n\t" \
    "ds_read_b64_tr_b16 v[90:91], v120 offset:13312\n\t" \
    "ds_read_b64_tr_b16 v[92:93], v121 offset:12288\n\t" \
    "v_lshlrev_b32 v106, 16, v65\n\t" \
    "v_and_b32 v107, 0xffff0000, v65\n\t" \
    "v_fmac_f32 v104, v106, v106\n\t" \
    "v_fmac_f32 v105, v107, v107\n\t" \
    "s_waitcnt lgkmcnt(4)\n\t" \
    "v_mfma_f32_32x32x16_bf16 v[32:47], v[72:75], v[68:71], v[32:47]\n\t" \
    "ds_read_b64_tr_b16 v[94:95], v121 offset:13312\n\t" \
    "ds_read_b128 v[84:87], v119 offset:0\n\t" \
    "v_lshlrev_b32 v106, 16, v66\n\t" \
    "v_and_b32 v107, 0xffff0000, v66\n\t" \
    "v_fmac_f32 v104, v106, v106\n\t" \
    "v_fmac_f32 v105, v107, v107\n\t" \
    "v_mfma_f32_16x16x32_bf16 v[96:99], v[108:111], v[72:75], v[96:99]\n\t" \
    "v_mfma_f32_32x32x16_bf16 v[48:63], v[76:79], v[68:71], v[48:63]\n\t" \
    "v_lshlrev_b32 v106, 16, v67\n\t" \
    "v_and_b32 v107, 0xffff0000, v67\n\t" \
    "v_fmac_f32 v104, v106, v106\n\t" \
    "v_fmac_f32 v105, v107, v107\n\t" \
    "v_mfma_f32_16x16x32_bf16 v[100:103], v[108:111], v[64:67], v[100:103]\n\t" \
    "s_waitcnt lgkmcnt(3)\n\t" \
    "v_mfma_f32_32x32x16_bf16 v[0:15], v[88:91], v[80:83], v[0:15]\n\t" \
    "s_waitcnt lgkmcnt(0)\n\t" \
    "s_barrier\n\t" \
    "ds_read_b128 v[64:67], v112 offset:32768\n\t" \
    "ds_read_b64_tr_b16 v[72:73], v120 offset:32768\n\t" \
    "v_lshlrev_b32 v106, 16, v80\n\t" \
    "v_and_b32 v107, 0xffff0000, v80\n\t" \
    "v_fmac_f32 v104, v106, v106\n\t" \
    "v_fmac_f32 v105, v107, v107\n\t" \
    "s_waitcnt lgkmcnt(3)\n\t" \
    "v_mfma_f32_32x32x16_bf16 v[16:31], v[92:95], v[80:83], v[16:31]\n\t" \
    "ds_read_b64_tr_b16 v[74:75], v120 offset:33792\n\t" \
    "ds_read_b64_tr_b16 v[76:77], v121 offset:32768\n\t" \
    "v_lshlrev_b32 v106, 16, v81\n\t" \
    "v_and_b32 v107, 0xffff0000, v81\n\t" \
    "v_fmac_f32 v104, v106, v106\n\t" \
    "v_fmac_f32 v105, v107, v107\n\t" \
    "s_waitcnt lgkmcnt(4)\n\t" \
    "v_mfma_f32_32x32x16_bf16 v[32:47], v[88:91], v[84:87], v[32:47]\n\t" \
    "ds_read_b64_tr_b16 v[78:79], v121 offset:33792\n\t" \
    "ds_read_b128 v[68:71], v116 offset:32768\n\t" \
    "v_lshlrev_b32 v106, 16, v82\n\t" \
    "v_and_b32 v107, 0xffff0000, v82\n\t" \
    "v_fmac_f32 v104, v106, v106\n\t" \
    "v_fmac_f32 v105, v107, v107\n\t" \
    "v_mfma_f32_16x16x32_bf16 v[96:99], v[108:111], v[88:91], v[96:99]\n\t" \
    "v_mfma_f32_32x32x16_bf16 v[48:63], v[92:95], v[84:87], v[48:63]\n\t" \
    "v_lshlrev_b32 v106, 16, v83\n\t" \
    "v_and_b32 v107, 0xffff0000, v83\n\t" \
    "v_fmac_f32 v104, v106, v106\n\t" \
    "v_fmac_f32 v105, v107, v107\n\t" \
    "v_mfma_f32_16x16x32_bf16 v[100:103], v[108:111], v[80:83], v[100:103]\n\t" \
    "s_waitcnt lgkmcnt(3)\n\t" \
    "v_mfma_f32_32x32x16_bf16 v[0:15], v[72:75], v[64:67], v[0:15]\n\t" \
    "ds_read_b128 v[80:83], v113 offset:32768\n\t" \
    "ds_read_b64_tr_b16 v[88:89], v120 offset:36864\n\t" \
    "v_lshlrev_b32 v106, 16, v64\n\t" \
    "v_and_b32 v107, 0xffff0000, v64\n\t" \
    "v_fmac_f32 v104, v106, v106\n\t" \
    "v_fmac_f32 v105, v107, v107\n\t" \
    "s_waitcnt lgkmcnt(3)\n\t" \
    "v_mfma_f32_32x32x16_bf16 v[16:31], v[76:79], v[64:67], v[16:31]\n\t" \
    "ds_read_b64_tr_b16 v[90:91], v120 offset:37888\n\t" \
    "ds_read_b64_tr_b16 v[92:93], v121 offset:36864\n\t" \
    "v_lshlrev_b32 v106, 16, v65\n\t" \
    "v_and_b32 v107, 0xffff0000, v65\n\t" \
    "v_fmac_f32 v104, v106, v106\n\t" \
    "v_fmac_f32 v105, v107, v107\n\t" \
    "s_waitcnt lgkmcnt(4)\n\t" \
    "v_mfma_f32_32x32x16_bf16 v[32:47], v[72:75], v[68:71], v[32:47]\n\t" \
    "ds_read_b64_tr_b16 v[94:95], v121 offset:37888\n\t" \
    "ds_read_b128 v[84:87], v117 offset:32768\n\t" \
    "v_lshlrev_b32 v106, 16, v66\n\t" \
    "v_and_b32 v107, 0xffff0000, v66\n\t" \
    "v_fmac_f32 v104, v106, v106\n\t" \
    "v_fmac_f32 v105, v107, v107\n\t" \
    "v_mfma_f32_16x16x32_bf16 v[96:99], v[108:111], v[72:75], v[96:99]\n\t" \
    "v_mfma_f32_32x32x16_bf16 v[48:63], v[76:79], v[68:71], v[48:63]\n\t" \
    "v_lshlrev_b32 v106, 16, v67\n\t" \
    "v_and_b32 v107, 0xffff0000, v67\n\t" \
    "v_fmac_f32 v104, v106, v106\n\t" \
    "v_fmac_f32 v105, v107, v107\n\t" \
    "v_mfma_f32_16x16x32_bf16 v[100:103], v[108:111], v[64:67], v[100:103]\n\t" \
    "s_waitcnt lgkmcnt(3)\n\t" \
    "v_mfma_f32_32x32x16_bf16 v[0:15], v[88:91], v[80:83], v[0:15]\n\t" \
    "ds_read_b128 v[64:67], v114 offset:32768\n\t" \
    "ds_read_b64_tr_b16 v[72:73], v120 offset:40960\n\t" \
    "v_lshlrev_b32 v106, 16, v80\n\t" \
    "v_and_b32 v107, 0xffff0000, v80\n\t" \
    "v_fmac_f32 v104, v106, v106\n\t" \
    "v_fmac_f32 v105, v107, v107\n\t" \
    "s_waitcnt lgkmcnt(3)\n\t" \
    "v_mfma_f32_32x32x16_bf16 v[16:31], v[92:95], v[80:83], v[16:31]\n\t" \
    "ds_read_b64_tr_b16 v[74:75], v120 offset:41984\n\t" \
    "ds_read_b64_tr_b16 v[76:77], v121 offset:40960\n\t" \
    "v_lshlrev_b32 v106, 16, v81\n\t" \
    "v_and_b32 v107, 0xffff0000, v81\n\t" \
    "v_fmac_f32 v104, v106, v106\n\t" \
    "v_fmac_f32 v105, v107, v107\n\t" \
    "s_waitcnt lgkmcnt(4)\n\t" \
    "v_mfma_f32_32x32x16_bf16 v[32:47], v[88:91], v[84:87], v[32:47]\n\t" \
    "ds_read_b64_tr_b16 v[78:79], v121 offset:41984\n\t" \
    "ds_read_b128 v[68:71], v118 offset:32768\n\t" \
    "v_lshlrev_b32 v106, 16, v82\n\t" \
    "v_and_b32 v107, 0xffff0000, v82\n\t" \
    "v_fmac_f32 v104, v106, v106\n\t" \
    "v_fmac_f32 v105, v107, v107\n\t" \
    "v_mfma_f32_16x16x32_bf16 v[96:99], v[108:111], v[88:91], v[96:99]\n\t" \
    "v_mfma_f32_32x32x16_bf16 v[48:63], v[92:95], v[84:87], v[48:63]\n\t" \
    "v_lshlrev_b32 v106, 16, v83\n\t" \
    "v_and_b32 v107, 0xffff0000, v83\n\t" \
    "v_fmac_f32 v104, v106, v106\n\t" \
    "v_fmac_f32 v105, v107, v107\n\t" \
    "v_mfma_f32_16x16x32_bf16 v[100:103], v[108:111], v[80:83], v[100:103]\n\t" \
    "s_waitcnt lgkmcnt(3)\n\t" \
    "v_mfma_f32_32x32x16_bf16 v[0:15], v[72:75], v[64:67], v[0:15]\n\t" \
    "ds_read_b128 v[80:83], v115 offset:32768\n\t" \
    "ds_read_b64_tr_b16 v[88:89], v120 offset:45056\n\t" \
    "v_lshlrev_b32 v106, 16, v64\n\t" \
    "v_and_b32 v107, 0xffff0000, v64\n\t" \
    "v_fmac_f32 v104, v106, v106\n\t" \
    "v_fmac_f32 v105, v107, v107\n\t" \
    "s_waitcnt lgkmcnt(3)\n\t" \
    "v_mfma_f32_32x32x16_bf16 v[16:31], v[76:79], v[64:67], v[16:31]\n\t" \
    "ds_read_b64_tr_b16 v[90:91], v120 offset:46080\n\t" \
    "ds_read_b64_tr_b16 v[92:93], v121 offset:45056\n\t" \
    "v_lshlrev_b32 v106, 16, v65\n\t" \
    "v_and_b32 v107, 0xffff0000, v65\n\t" \
    "v_fmac_f32 v104, v106, v106\n\t" \
    "v_fmac_f32 v105, v107, v107\n\t" \
    "s_waitcnt lgkmcnt(4)\n\t" \
    "v_mfma_f32_32x32x16_bf16 v[32:47], v[72:75], v[68:71], v[32:47]\n\t" \
    "ds_read_b64_tr_b16 v[94:95], v121 offset:46080\n\t" \
    "ds_read_b128 v[84:87], v119 offset:32768\n\t" \
    "v_lshlrev_b32 v106, 16, v66\n\t" \
    "v_and_b32 v107, 0xffff0000, v66\n\t" \
    "v_fmac_f32 v104, v106, v106\n\t" \
    "v_fmac_f32 v105, v107, v107\n\t" \
    "v_mfma_f32_16x16x32_bf16 v[96:99], v[108:111], v[72:75], v[96:99]\n\t" \
    "v_mfma_f32_32x32x16_bf16 v[48:63], v[76:79], v[68:71], v[48:63]\n\t" \
    "v_lshlrev_b32 v106, 16, v67\n\t" \
    "v_and_b32 v107, 0xffff0000, v67\n\t" \
    "v_fmac_f32 v104, v106, v106\n\t" \
    "v_fmac_f32 v105, v107, v107\n\t" \
    "v_mfma_f32_16x16x32_bf16 v[100:103], v[108:111], v[64:67], v[100:103]\n\t" \
    "s_waitcnt lgkmcnt(3)\n\t" \
    "v_mfma_f32_32x32x16_bf16 v[0:15], v[88:91], v[80:83], v[0:15]\n\t" \
    "s_waitcnt lgkmcnt(0)\n\t" \
    "s_barrier\n\t" \
    "v_xor_b32 v112, 0x10000, v112\n\t" \
    "v_xor_b32 v113, 0x10000, v113\n\t" \
    "v_xor_b32 v114, 0x10000, v114\n\t" \
    "v_xor_b32 v115, 0x10000, v115\n\t" \
    "v_xor_b32 v116, 0x10000, v116\n\t" \
    "v_xor_b32 v117, 0x10000, v117\n\t" \
    "v_xor_b32 v118, 0x10000, v118\n\t" \
    "v_xor_b32 v119, 0x10000, v119\n\t" \
    "v_xor_b32 v120, 0x10000, v120\n\t" \
    "v_xor_b32 v121, 0x10000, v121\n\t" \
    "ds_read_b128 v[64:67], v112 offset:0\n\t" \
    "ds_read_b64_tr_b16 v[72:73], v120 offset:0\n\t" \
    "v_lshlrev_b32 v106, 16, v80\n\t" \
    "v_and_b32 v107, 0xffff0000, v80\n\t" \
    "v_fmac_f32 v104, v106, v106\n\t" \
    "v_fmac_f32 v105, v107, v107\n\t" \
    "s_waitcnt lgkmcnt(3)\n\t" \
    "v_mfma_f32_32x32x16_bf16 v[16:31], v[92:95], v[80:83], v[16:31]\n\t" \
    "ds_read_b64_tr_b16 v[74:75], v120 offset:1024\n\t" \
    "ds_read_b64_tr_b16 v[76:77], v121 offset:0\n\t" \
    "v_lshlrev_b32 v106, 16, v81\n\t" \
    "v_and_b32 v107, 0xffff0000, v81\n\t" \
    "v_fmac_f32 v104, v106, v106\n\t" \
    "v_fmac_f32 v105, v107, v107\n\t" \
    "s_waitcnt lgkmcnt(4)\n\t" \
    "v_mfma_f32_32x32x16_bf16 v[32:47], v[88:91], v[84:87], v[32:47]\n\t" \
    "ds_read_b64_tr_b16 v[78:79], v121 offset:1024\n\t" \
    "ds_read_b128 v[68:71], v116 offset:0\n\t" \
    "v_lshlrev_b32 v106, 16, v82\n\t" \
    "v_and_b32 v107, 0xffff0000, v82\n\t" \
    "v_fmac_f32 v104, v106, v106\n\t" \
    "v_fmac_f32 v105, v107, v107\n\t" \
    "v_mfma_f32_16x16x32_bf16 v[96:99], v[108:111], v[88:91], v[96:99]\n\t" \
    "v_mfma_f32_32x32x16_bf16 v[48:63], v[92:95], v[84:87], v[48:63]\n\t" \
    "v_lshlrev_b32 v106, 16, v83\n\t" \
    "v_and_b32 v107, 0xffff0000, v83\n\t" \
    "v_fmac_f32 v104, v106, v106\n\t" \
    "v_fmac_f32 v105, v107, v107\n\t" \
    "v_mfma_f32_16x16x32_bf16 v[100:103], v[108:111], v[80:83], v[100:103]\n\t" \
    "s_sub_u32 %0, %0, 1\n\t" \
    "s_cmp_lg_u32 %0, 0\n\t" \
    "s_cbranch_scc1 1b\n\t" \
    "s_waitcnt lgkmcnt(0)\n\t" \
    "s_nop 15\n\t"
#define G_LOOP_V \
    "s_barrier\n\t" \
    "ds_read_b128 v[64:67], v112\n\t" \
    "ds_read_b64_tr_b16 v[72:73], v120\n\t" \
    "ds_read_b64_tr_b16 v[74:75], v120 offset:1024\n\t" \
    "ds_read_b64_tr_b16 v[76:77], v121\n\t" \
    "ds_read_b64_tr_b16 v[78:79], v121 offset:1024\n\t" \
    "ds_read_b128 v[68:71], v116\n\t" \
    "s_mov_b32 %0, 5\n\t" \
    "1:\n\t" \
    "s_waitcnt lgkmcnt(3)\n\t" \
    "v_mfma_f32_32x32x16_bf16 v[0:15], v[64:67], v[72:75], v[0:15]\n\t" \
    "ds_read_b128 v[80:83], v113 offset:0\n\t" \
    "ds_read_b64_tr_b16 v[88:89], v120 offset:4096\n\t" \
    "v_lshlrev_b32 v106, 16, v64\n\t" \
    "v_and_b32 v107, 0xffff0000, v64\n\t" \
    "v_fmac_f32 v104, v106, v106\n\t" \
    "v_fmac_f32 v105, v107, v107\n\t" \
    "s_waitcnt lgkmcnt(3)\n\t" \
    "v_mfma_f32_32x32x16_bf16 v[16:31], v[64:67], v[76:79], v[16:31]\n\t" \
    "ds_read_b64_tr_b16 v[90:91], v120 offset:5120\n\t" \
    "ds_read_b64_tr_b16 v[92:93], v121 offset:4096\n\t" \
    "v_lshlrev_b32 v106, 16, v65\n\t" \
    "v_and_b32 v107, 0xffff0000, v65\n\t" \
    "v_fmac_f32 v104, v106, v106\n\t" \
    "v_fmac_f32 v105, v107, v107\n\t" \
    "s_waitcnt lgkmcnt(4)\n\t" \
    "v_mfma_f32_32x32x16_bf16 v[32:47], v[68:71], v[72:75], v[32:47]\n\t" \
    "ds_read_b64_tr_b16 v[94:95], v121 offset:5120\n\t" \
    "ds_read_b128 v[84:87], v117 offset:0\n\t" \
    "v_lshlrev_b32 v106, 16, v66\n\t" \
    "v_and_b32 v107, 0xffff0000, v66\n\t" \
    "v_fmac_f32 v104, v106, v106\n\t" \
    "v_fmac_f32 v105, v107, v107\n\t" \
    "v_mfma_f32_16x16x32_bf16 v[96:99], v[108:111], v[72:75], v[96:99]\n\t" \
    "v_mfma_f32_32x32x16_bf16 v[48:63], v[68:71], v[76:79], v[48:63]\n\t" \
    "v_lshlrev_b32 v106, 16, v67\n\t" \
    "v_and_b32 v107, 0xffff0000, v67\n\t" \
    "v_fmac_f32 v104, v106, v106\n\t" \
    "v_fmac_f32 v105, v107, v107\n\t" \
    "v_mfma_f32_16x16x32_bf16 v[100:103], v[108:111], v[64:67], v[100:103]\n\t" \
    "s_waitcnt lgkmcnt(3)\n\t" \
    "v_mfma_f32_32x32x16_bf16 v[0:15], v[80:83], v[88:91], v[0:15]\n\t" \
    "ds_read_b128 v[64:67], v114 offset:0\n\t" \
    "ds_read_b64_tr_b16 v[72:73], v120 offset:8192\n\t" \
    "v_lshlrev_b32 v106, 16, v80\n\t" \
    "v_and_b32 v107, 0xffff0000, v80\n\t" \
    "v_fmac_f32 v104, v106, v106\n\t" \
    "v_fmac_f32 v105, v107, v107\n\t" \
    "s_waitcnt lgkmcnt(3)\n\t" \
    "v_mfma_f32_32x32x16_bf16 v[16:31], v[80:83], v[92:95], v[16:31]\n\t" \
    "ds_read_b64_tr_b16 v[74:75], v120 offset:9216\n\t" \
    "ds_read_b64_tr_b16 v[76:77], v121 offset:8192\n\t" \
    "v_lshlrev_b32 v106, 16, v81\n\t" \
    "v_and_b32 v107, 0xffff0000, v81\n\t" \
    "v_fmac_f32 v104, v106, v106\n\t" \
    "v_fmac_f32 v105, v107, v107\n\t" \
    "s_waitcnt lgkmcnt(4)\n\t" \
    "v_mfma_f32_32x32x16_bf16 v[32:47], v[84:87], v[88:91], v[32:47]\n\t" \
    "ds_read_b64_tr_b16 v[78:79], v121 offset:9216\n\t" \
    "ds_read_b128 v[68:71], v118 offset:0\n\t" \
    "v_lshlrev_b32 v106, 16, v82\n\t" \
    "v_and_b32 v107, 0xffff0000, v82\n\t" \
    "v_fmac_f32 v104, v106, v106\n\t" \
    "v_fmac_f32 v105, v107, v107\n\t" \
    "v_mfma_f32_16x16x32_bf16 v[96:99], v[108:111], v[88:91], v[96:99]\n\t" \
    "v_mfma_f32_32x32x16_bf16 v[48:63], v[84:87], v[92:95], v[48:63]\n\t" \
    "v_lshlrev_b32 v106, 16, v83\n\t" \
    "v_and_b32 v107, 0xffff0000, v83\n\t" \
    "v_fmac_f32 v104, v106, v106\n\t" \
    "v_fmac_f32 v105, v107, v107\n\t" \
    "v_mfma_f32_16x16x32_bf16 v[100:103], v[108:111], v[80:83], v[100:103]\n\t" \
    "s_waitcnt lgkmcnt(3)\n\t" \
    "v_mfma_f32_32x32x16_bf16 v[0:15], v[64:67], v[72:75], v[0:15]\n\t" \
    "ds_read_b128 v[80:83], v115 offset:0\n\t" \
    "ds_read_b64_tr_b16 v[88:89], v120 offset:12288\n\t" \
    "v_lshlrev_b32 v106, 16, v64\n\t" \
    "v_and_b32 v107, 0xffff0000, v64\n\t" \
    "v_fmac_f32 v104, v106, v106\n\t" \
    "v_fmac_f32 v105, v107, v107\n\t" \
    "s_waitcnt lgkmcnt(3)\n\t" \
    "v_mfma_f32_32x32x16_bf16 v[16:31], v[64:67], v[76:79], v[16:31]\n\t" \
    "ds_read_b64_tr_b16 v[90:91], v120 offset:13312\n\t" \
    "ds_read_b64_tr_b16 v[92:93], v121 offset:12288\n\t" \
    "v_lshlrev_b32 v106, 16, v65\n\t" \
    "v_and_b32 v107, 0xffff0000, v65\n\t" \
    "v_fmac_f32 v104, v106, v106\n\t" \
    "v_fmac_f32 v105, v107, v107\n\t" \
    "s_waitcnt lgkmcnt(4)\n\t" \
    "v_mfma_f32_32x32x16_bf16 v[32:47], v[68:71], v[72:75], v[32:47]\n\t" \
    "ds_read_b64_tr_b16 v[94:95], v121 offset:13312\n\t" \
    "ds_read_b128 v[84:87], v119 offset:0\n\t" \
    "v_lshlrev_b32 v106, 16, v66\n\t" \
    "v_and_b32 v107, 0xffff0000, v66\n\t" \
    "v_fmac_f32 v104, v106, v106\n\t" \
    "v_fmac_f32 v105, v107, v107\n\t" \
    "v_mfma_f32_16x16x32_bf16 v[96:99], v[108:111], v[72:75], v[96:99]\n\t" \
    "v_mfma_f32_32x32x16_bf16 v[48:63], v[68:71], v[76:79], v[48:63]\n\t" \
    "v_lshlrev_b32 v106, 16, v67\n\t" \
    "v_and_b32 v107, 0xffff0000, v67\n\t" \
    "v_fmac_f32 v104, v106, v106\n\t" \
    "v_fmac_f32 v105, v107, v107\n\t" \
    "v_mfma_f32_16x16x32_bf16 v[100:103], v[108:111], v[64:67], v[100:103]\n\t" \
    "s_waitcnt lgkmcnt(3)\n\t" \
    "v_mfma_f32_32x32x16_bf16 v[0:15], v[80:83], v[88:91], v[0:15]\n\t" \
    "s_waitcnt lgkmcnt(0)\n\t" \
    "s_barrier\n\t" \
    "ds_read_b128 v[64:67], v112 offset:32768\n\t" \
    "ds_read_b64_tr_b16 v[72:73], v120 offset:32768\n\t" \
    "v_lshlrev_b32 v106, 16, v80\n\t" \
    "v_and_b32 v107, 0xffff0000, v80\n\t" \
    "v_fmac_f32 v104, v106, v106\n\t" \
    "v_fmac_f32 v105, v107, v107\n\t" \
    "s_waitcnt lgkmcnt(3)\n\t" \
    "v_mfma_f32_32x32x16_bf16 v[16:31], v[80:83], v[92:95], v[16:31]\n\t" \
    "ds_read_b64_tr_b16 v[74:75], v120 offset:33792\n\t" \
    "ds_read_b64_tr_b16 v[76:77], v121 offset:32768\n\t" \
    "v_lshlrev_b32 v106, 16, v81\n\t" \
    "v_and_b32 v107, 0xffff0000, v81\n\t" \
    "v_fmac_f32 v104, v106, v106\n\t" \
    "v_fmac_f32 v105, v107, v107\n\t" \
    "s_waitcnt lgkmcnt(4)\n\t" \
    "v_mfma_f32_32x32x16_bf16 v[32:47], v[84:87], v[88:91], v[32:47]\n\t" \
    "ds_read_b64_tr_b16 v[78:79], v121 offset:33792\n\t" \
    "ds_read_b128 v[68:71], v116 offset:32768\n\t" \
    "v_lshlrev_b32 v106, 16, v82\n\t" \
    "v_and_b32 v107, 0xffff0000, v82\n\t" \
    "v_fmac_f32 v104, v106, v106\n\t" \
    "v_fmac_f32 v105, v107, v107\n\t" \
    "v_mfma_f32_16x16x32_bf16 v[96:99], v[108:111], v[88:91], v[96:99]\n\t" \
    "v_mfma_f32_32x32x16_bf16 v[48:63], v[84:87], v[92:95], v[48:63]\n\t" \
    "v_lshlrev_b32 v106, 16, v83\n\t" \
    "v_and_b32 v107, 0xffff0000, v83\n\t" \
    "v_fmac_f32 v104, v106, v106\n\t" \
    "v_fmac_f32 v105, v107, v107\n\t" \
    "v_mfma_f32_16x16x32_bf16 v[100:103], v[108:111], v[80:83], v[100:103]\n\t" \
    "s_waitcnt lgkmcnt(3)\n\t" \
    "v_mfma_f32_32x32x16_bf16 v[0:15], v[64:67], v[72:75], v[0:15]\n\t" \
    "ds_read_b128 v[80:83], v113 offset:32768\n\t" \
    "ds_read_b64_tr_b16 v[88:89], v120 offset:36864\n\t" \
    "v_lshlrev_b32 v106, 16, v64\n\t" \
    "v_and_b32 v107, 0xffff0000, v64\n\t" \
    "v_fmac_f32 v104, v106, v106\n\t" \
    "v_fmac_f32 v105, v107, v107\n\t" \
    "s_waitcnt lgkmcnt(3)\n\t" \
    "v_mfma_f32_32x32x16_bf16 v[16:31], v[64:67], v[76:79], v[16:31]\n\t" \
    "ds_read_b64_tr_b16 v[90:91], v120 offset:37888\n\t" \
    "ds_read_b64_tr_b16 v[92:93], v121 offset:36864\n\t" \
    "v_lshlrev_b32 v106, 16, v65\n\t" \
    "v_and_b32 v107, 0xffff0000, v65\n\t" \
    "v_fmac_f32 v104, v106, v106\n\t" \
    "v_fmac_f32 v105, v107, v107\n\t" \
    "s_waitcnt lgkmcnt(4)\n\t" \
    "v_mfma_f32_32x32x16_bf16 v[32:47], v[68:71], v[72:75], v[32:47]\n\t" \
    "ds_read_b64_tr_b16 v[94:95], v121 offset:37888\n\t" \
    "ds_read_b128 v[84:87], v117 offset:32768\n\t" \
    "v_lshlrev_b32 v106, 16, v66\n\t" \
    "v_and_b32 v107, 0xffff0000, v66\n\t" \
    "v_fmac_f32 v104, v106, v106\n\t" \
    "v_fmac_f32 v105, v107, v107\n\t" \
    "v_mfma_f32_16x16x32_bf16 v[96:99], v[108:111], v[72:75], v[96:99]\n\t" \
    "v_mfma_f32_32x32x16_bf16 v[48:63], v[68:71], v[76:79], v[48:63]\n\t" \
    "v_lshlrev_b32 v106, 16, v67\n\t" \
    "v_and_b32 v107, 0xffff0000, v67\n\t" \
    "v_fmac_f32 v104, v106, v106\n\t" \
    "v_fmac_f32 v105, v107, v107\n\t" \
    "v_mfma_f32_16x16x32_bf16 v[100:103], v[108:111], v[64:67], v[100:103]\n\t" \
    "s_waitcnt lgkmcnt(3)\n\t" \
    "v_mfma_f32_32x32x16_bf16 v[0:15], v[80:83], v[88:91], v[0:15]\n\t" \
    "ds_read_b128 v[64:67], v114 offset:32768\n\t" \
    "ds_read_b64_tr_b16 v[72:73], v120 offset:40960\n\t" \
    "v_lshlrev_b32 v106, 16, v80\n\t" \
    "v_and_b32 v107, 0xffff0000, v80\n\t" \
    "v_fmac_f32 v104, v106, v106\n\t" \
    "v_fmac_f32 v105, v107, v107\n\t" \
    "s_waitcnt lgkmcnt(3)\n\t" \
    "v_mfma_f32_32x32x16_bf16 v[16:31], v[80:83], v[92:95], v[16:31]\n\t" \
    "ds_read_b64_tr_b16 v[74:75], v120 offset:41984\n\t" \
    "ds_read_b64_tr_b16 v[76:77], v121 offset:40960\n\t" \
    "v_lshlrev_b32 v106, 16, v81\n\t" \
    "v_and_b32 v107, 0xffff0000, v81\n\t" \
    "v_fmac_f32 v104, v106, v106\n\t" \
    "v_fmac_f32 v105, v107, v107\n\t" \
    "s_waitcnt lgkmcnt(4)\n\t" \
    "v_mfma_f32_32x32x16_bf16 v[32:47], v[84:87], v[88:91], v[32:47]\n\t" \
    "ds_read_b64_tr_b16 v[78:79], v121 offset:41984\n\t" \
    "ds_read_b128 v[68:71], v118 offset:32768\n\t" \
    "v_lshlrev_b32 v106, 16, v82\n\t" \
    "v_and_b32 v107, 0xffff0000, v82\n\t" \
    "v_fmac_f32 v104, v106, v106\n\t" \
    "v_fmac_f32 v105, v107, v107\n\t" \
    "v_mfma_f32_16x16x32_bf16 v[96:99], v[108:111], v[88:91], v[96:99]\n\t" \
    "v_mfma_f32_32x32x16_bf16 v[48:63], v[84:87], v[92:95], v[48:63]\n\t" \
    "v_lshlrev_b32 v106, 16, v83\n\t" \
    "v_and_b32 v107, 0xffff0000, v83\n\t" \
    "v_fmac_f32 v104, v106, v106\n\t" \
    "v_fmac_f32 v105, v107, v107\n\t" \
    "v_mfma_f32_16x16x32_bf16 v[100:103], v[108:111], v[80:83], v[100:103]\n\t" \
    "s_waitcnt lgkmcnt(3)\n\t" \
    "v_mfma_f32_32x32x16_bf16 v[0:15], v[64:67], v[72:75], v[0:15]\n\t" \
    "ds_read_b128 v[80:83], v115 offset:32768\n\t" \
    "ds_read_b64_tr_b16 v[88:89], v120 offset:45056\n\t" \
    "v_lshlrev_b32 v106, 16, v64\n\t" \
    "v_and_b32 v107, 0xffff0000, v64\n\t" \
    "v_fmac_f32 v104, v106, v106\n\t" \
    "v_fmac_f32 v105, v107, v107\n\t" \
    "s_waitcnt lgkmcnt(3)\n\t" \
    "v_mfma_f32_32x32x16_bf16 v[16:31], v[64:67], v[76:79], v[16:31]\n\t" \
    "ds_read_b64_tr_b16 v[90:91], v120 offset:46080\n\t" \
    "ds_read_b64_tr_b16 v[92:93], v121 offset:45056\n\t" \
    "v_lshlrev_b32 v106, 16, v65\n\t" \
    "v_and_b32 v107, 0xffff0000, v65\n\t" \
    "v_fmac_f32 v104, v106, v106\n\t" \
    "v_fmac_f32 v105, v107, v107\n\t" \
    "s_waitcnt lgkmcnt(4)\n\t" \
    "v_mfma_f32_32x32x16_bf16 v[32:47], v[68:71], v[72:75], v[32:47]\n\t" \
    "ds_read_b64_tr_b16 v[94:95], v121 offset:46080\n\t" \
    "ds_read_b128 v[84:87], v119 offset:32768\n\t" \
    "v_lshlrev_b32 v106, 16, v66\n\t" \
    "v_and_b32 v107, 0xffff0000, v66\n\t" \
    "v_fmac_f32 v104, v106, v106\n\t" \
    "v_fmac_f32 v105, v107, v107\n\t" \
    "v_mfma_f32_16x16x32_bf16 v[96:99], v[108:111], v[72:75], v[96:99]\n\t" \
    "v_mfma_f32_32x32x16_bf16 v[48:63], v[68:71], v[76:79], v[48:63]\n\t" \
    "v_lshlrev_b32 v106, 16, v67\n\t" \
    "v_and_b32 v107, 0xffff0000, v67\n\t" \
    "v_fmac_f32 v104, v106, v106\n\t" \
    "v_fmac_f32 v105, v107, v107\n\t" \
    "v_mfma_f32_16x16x32_bf16 v[100:103], v[108:111], v[64:67], v[100:103]\n\t" \
    "s_waitcnt lgkmcnt(3)\n\t" \
    "v_mfma_f32_32x32x16_bf16 v[0:15], v[80:83], v[88:91], v[0:15]\n\t" \
    "s_waitcnt lgkmcnt(0)\n\t" \
    "s_barrier\n\t" \
    "v_xor_b32 v112, 0x10000, v112\n\t" \
    "v_xor_b32 v113, 0x10000, v113\n\t" \
    "v_xor_b32 v114, 0x10000, v114\n\t" \
    "v_xor_b32 v115, 0x10000, v115\n\t" \
    "v_xor_b32 v116, 0x10000, v116\n\t" \
    "v_xor_b32 v117, 0x10000, v117\n\t" \
    "v_xor_b32 v118, 0x10000, v118\n\t" \
    "v_xor_b32 v119, 0x10000, v119\n\t" \
    "v_xor_b32 v120, 0x10000, v120\n\t" \
    "v_xor_b32 v121, 0x10000, v121\n\t" \
    "ds_read_b128 v[64:67], v112 offset:0\n\t" \
    "ds_read_b64_tr_b16 v[72:73], v120 offset:0\n\t" \
    "v_lshlrev_b32 v106, 16, v80\n\t" \
    "v_and_b32 v107, 0xffff0000, v80\n\t" \
    "v_fmac_f32 v104, v106, v106\n\t" \
    "v_fmac_f32 v105, v107, v107\n\t" \
    "s_waitcnt lgkmcnt(3)\n\t" \
    "v_mfma_f32_32x32x16_bf16 v[16:31], v[80:83], v[92:95], v[16:31]\n\t" \
    "ds_read_b64_tr_b16 v[74:75], v120 offset:1024\n\t" \
    "ds_read_b64_tr_b16 v[76:77], v121 offset:0\n\t" \
    "v_lshlrev_b32 v106, 16, v81\n\t" \
    "v_and_b32 v107, 0xffff0000, v81\n\t" \
    "v_fmac_f32 v104, v106, v106\n\t" \
    "v_fmac_f32 v105, v107, v107\n\t" \
    "s_waitcnt lgkmcnt(4)\n\t" \
    "v_mfma_f32_32x32x16_bf16 v[32:47], v[84:87], v[88:91], v[32:47]\n\t" \
    "ds_read_b64_tr_b16 v[78:79], v121 offset:1024\n\t" \
    "ds_read_b128 v[68:71], v116 offset:0\n\t" \
    "v_lshlrev_b32 v106, 16, v82\n\t" \
    "v_and_b32 v107, 0xffff0000, v82\n\t" \
    "v_fmac_f32 v104, v106, v106\n\t" \
    "v_fmac_f32 v105, v107, v107\n\t" \
    "v_mfma_f32_16x16x32_bf16 v[96:99], v[108:111], v[88:91], v[96:99]\n\t" \
    "v_mfma_f32_32x32x16_bf16 v[48:63], v[84:87], v[92:95], v[48:63]\n\t" \
    "v_lshlrev_b32 v106, 16, v83\n\t" \
    "v_and_b32 v107, 0xffff0000, v83\n\t" \
    "v_fmac_f32 v104, v106, v106\n\t" \
    "v_fmac_f32 v105, v107, v107\n\t" \
    "v_mfma_f32_16x16x32_bf16 v[100:103], v[108:111], v[80:83], v[100:103]\n\t" \
    "s_sub_u32 %0, %0, 1\n\t" \
    "s_cmp_lg_u32 %0, 0\n\t" \
    "s_cbranch_scc1 1b\n\t" \
    "s_waitcnt lgkmcnt(0)\n\t" \
    "s_nop 15\n\t"
#define FFN1_LOOP \
    "buffer_load_dwordx4 v[116:119], v112, %[srd], %[soff] offen sc1\n\t" \
    "s_add_u32 %[soff], %[soff], 1024\n\t" \
    "buffer_load_dwordx4 v[120:123], v112, %[srd], %[soff] offen sc1\n\t" \
    "s_add_u32 %[soff], %[soff], 1024\n\t" \
    "buffer_load_dwordx4 v[124:127], v112, %[srd], %[soff] offen sc1\n\t" \
    "s_add_u32 %[soff], %[soff], 1024\n\t" \
    "buffer_load_dwordx4 v[128:131], v112, %[srd], %[soff] offen sc1\n\t" \
    "s_add_u32 %[soff], %[soff], 1024\n\t" \
    "buffer_load_dwordx4 v[132:135], v112, %[srd], %[soff] offen sc1\n\t" \
    "s_add_u32 %[soff], %[soff], 1024\n\t" \
    "buffer_load_dwordx4 v[136:139], v112, %[srd], %[soff] offen sc1\n\t" \
    "s_add_u32 %[soff], %[soff], 1024\n\t" \
    "buffer_load_dwordx4 v[140:143], v112, %[srd], %[soff] offen sc1\n\t" \
    "s_add_u32 %[soff], %[soff], 1024\n\t" \
    "buffer_load_dwordx4 v[144:147], v112, %[srd], %[soff] offen sc1\n\t" \
    "s_add_u32 %[soff], %[soff], 1024\n\t" \
    "buffer_load_dwordx4 v[148:151], v112, %[srd], %[soff] offen sc1\n\t" \
    "s_add_u32 %[soff], %[soff], 1024\n\t" \
    "buffer_load_dwordx4 v[152:155], v112, %[srd], %[soff] offen sc1\n\t" \
    "s_add_u32 %[soff], %[soff], 1024\n\t" \
    "s_barrier\n\t" \
    "ds_read_b64_tr_b16 v[64:65], v104 offset:0\n\t" \
    "ds_read_b64_tr_b16 v[66:67], v104 offset:1024\n\t" \
    "ds_read_b64_tr_b16 v[68:69], v105 offset:0\n\t" \
    "ds_read_b64_tr_b16 v[70:71], v105 offset:1024\n\t" \
    "ds_read_b64_tr_b16 v[72:73], v106 offset:0\n\t" \
    "ds_read_b64_tr_b16 v[74:75], v106 offset:1024\n\t" \
    "ds_read_b64_tr_b16 v[76:77], v107 offset:0\n\t" \
    "ds_read_b64_tr_b16 v[78:79], v107 offset:1024\n\t" \
    "s_waitcnt vmcnt(9) lgkmcnt(6)\n\t" \
    "v_mfma_f32_32x32x16_bf16 v[0:15], v[64:67], v[116:119], v[0:15]\n\t" \
    "ds_read_b64_tr_b16 v[80:81], v104 offset:4096\n\t" \
    "ds_read_b64_tr_b16 v[82:83], v104 offset:5120\n\t" \
    "s_waitcnt lgkmcnt(6)\n\t" \
    "v_mfma_f32_32x32x16_bf16 v[16:31], v[68:71], v[116:119], v[16:31]\n\t" \
    "ds_read_b64_tr_b16 v[84:85], v105 offset:4096\n\t" \
    "ds_read_b64_tr_b16 v[86:87], v105 offset:5120\n\t" \
    "buffer_load_dwordx4 v[156:159], v112, %[srd], %[soff] offen sc1\n\t" \
    "s_waitcnt lgkmcnt(6)\n\t" \
    "v_mfma_f32_32x32x16_bf16 v[32:47], v[72:75], v[116:119], v[32:47]\n\t" \
    "ds_read_b64_tr_b16 v[88:89], v106 offset:4096\n\t" \
    "ds_read_b64_tr_b16 v[90:91], v106 offset:5120\n\t" \
    "s_add_u32 %[soff], %[soff], 1024\n\t" \
    "s_waitcnt lgkmcnt(6)\n\t" \
    "v_mfma_f32_32x32x16_bf16 v[48:63], v[76:79], v[116:119], v[48:63]\n\t" \
    "ds_read_b64_tr_b16 v[92:93], v107 offset:4096\n\t" \
    "ds_read_b64_tr_b16 v[94:95], v107 offset:5120\n\t" \
    "s_waitcnt vmcnt(9) lgkmcnt(6)\n\t" \
    "v_mfma_f32_32x32x16_bf16 v[0:15], v[80:83], v[120:123], v[0:15]\n\t" \
    "ds_read_b64_tr_b16 v[64:65], v104 offset:8192\n\t" \
    "ds_read_b64_tr_b16 v[66:67], v104 offset:9216\n\t" \
    "s_waitcnt lgkmcnt(6)\n\t" \
    "v_mfma_f32_32x32x16_bf16 v[16:31], v[84:87], v[120:123], v[16:31]\n\t" \
    "ds_read_b64_tr_b16 v[68:69], v105 offset:8192\n\t" \
    "ds_read_b64_tr_b16 v[70:71], v105 offset:9216\n\t" \
    "buffer_load_dwordx4 v[160:163], v112, %[srd], %[soff] offen sc1\n\t" \
    "s_waitcnt lgkmcnt(6)\n\t" \
    "v_mfma_f32_32x32x16_bf16 v[32:47], v[88:91], v[120:123], v[32:47]\n\t" \
    "ds_read_b64_tr_b16 v[72:73], v106 offset:8192\n\t" \
    "ds_read_b64_tr_b16 v[74:75], v106 offset:9216\n\t" \
    "s_add_u32 %[soff], %[soff], 1024\n\t" \
    "s_waitcnt lgkmcnt(6)\n\t" \
    "v_mfma_f32_32x32x16_bf16 v[48:63], v[92:95], v[120:123], v[48:63]\n\t" \
    "ds_read_b64_tr_b16 v[76:77], v107 offset:8192\n\t" \
    "ds_read_b64_tr_b16 v[78:79], v107 offset:9216\n\t" \
    "s_waitcnt vmcnt(9) lgkmcnt(6)\n\t" \
    "v_mfma_f32_32x32x16_bf16 v[0:15], v[64:67], v[124:127], v[0:15]\n\t" \
    "ds_read_b64_tr_b16 v[80:81], v104 offset:12288\n\t" \
    "ds_read_b64_tr_b16 v[82:83], v104 offset:13312\n\t" \
    "s_waitcnt lgkmcnt(6)\n\t" \
    "v_mfma_f32_32x32x16_bf16 v[16:31], v[68:71], v[124:127], v[16:31]\n\t" \
    "ds_read_b64_tr_b16 v[84:85], v105 offset:12288\n\t" \
    "ds_read_b64_tr_b16 v[86:87], v105 offset:13312\n\t" \
    "buffer_load_dwordx4 v[116:119], v112, %[srd], %[soff] offen sc1\n\t" \
    "s_waitcnt lgkmcnt(6)\n\t" \
    "v_mfma_f32_32x32x16_bf16 v[32:47], v[72:75], v[124:127], v[32:47]\n\t" \
    "ds_read_b64_tr_b16 v[88:89], v106 offset:12288\n\t" \
    "ds_read_b64_tr_b16 v[90:91], v106 offset:13312\n\t" \
    "s_add_u32 %[soff], %[soff], 1024\n\t" \
    "s_waitcnt lgkmcnt(6)\n\t" \
    "v_mfma_f32_32x32x16_bf16 v[48:63], v[76:79], v[124:127], v[48:63]\n\t" \
    "ds_read_b64_tr_b16 v[92:93], v107 offset:12288\n\t" \
    "ds_read_b64_tr_b16 v[94:95], v107 offset:13312\n\t" \
    "s_waitcnt vmcnt(9) lgkmcnt(6)\n\t" \
    "v_mfma_f32_32x32x16_bf16 v[0:15], v[80:83], v[128:131], v[0:15]\n\t" \
    "s_waitcnt lgkmcnt(0)\n\t" \
    "s_barrier\n\t" \
    "ds_read_b64_tr_b16 v[64:65], v104 offset:16384\n\t" \
    "ds_read_b64_tr_b16 v[66:67], v104 offset:17408\n\t" \
    "s_waitcnt lgkmcnt(6)\n\t" \
    "v_mfma_f32_32x32x16_bf16 v[16:31], v[84:87], v[128:131], v[16:31]\n\t" \
    "ds_read_b64_tr_b16 v[68:69], v105 offset:16384\n\t" \
    "ds_read_b64_tr_b16 v[70:71], v105 offset:17408\n\t" \
    "buffer_load_dwordx4 v[120:123], v112, %[srd], %[soff] offen sc1\n\t" \
    "s_waitcnt lgkmcnt(6)\n\t" \
    "v_mfma_f32_32x32x16_bf16 v[32:47], v[88:91], v[128:131], v[32:47]\n\t" \
    "ds_read_b64_tr_b16 v[72:73], v106 offset:16384\n\t" \
    "ds_read_b64_tr_b16 v[74:75], v106 offset:17408\n\t" \
    "s_add_u32 %[soff], %[soff], 1024\n\t" \
    "s_waitcnt lgkmcnt(6)\n\t" \
    "v_mfma_f32_32x32x16_bf16 v[48:63], v[92:95], v[128:131], v[48:63]\n\t" \
    "ds_read_b64_tr_b16 v[76:77], v107 offset:16384\n\t" \
    "ds_read_b64_tr_b16 v[78:79], v107 offset:17408\n\t" \
    "s_waitcnt vmcnt(9) lgkmcnt(6)\n\t" \
    "v_mfma_f32_32x32x16_bf16 v[0:15], v[64:67], v[132:135], v[0:15]\n\t" \
    "ds_read_b64_tr_b16 v[80:81], v104 offset:20480\n\t" \
    "ds_read_b64_tr_b16 v[82:83], v104 offset:21504\n\t" \
    "s_waitcnt lgkmcnt(6)\n\t" \
    "v_mfma_f32_32x32x16_bf16 v[16:31], v[68:71], v[132:135], v[16:31]\n\t" \
    "ds_read_b64_tr_b16 v[84:85], v105 offset:20480\n\t" \
    "ds_read_b64_tr_b16 v[86:87], v105 offset:21504\n\t" \
    "buffer_load_dwordx4 v[124:127], v112, %[srd], %[soff] offen sc1\n\t" \
    "s_waitcnt lgkmcnt(6)\n\t" \
    "v_mfma_f32_32x32x16_bf16 v[32:47], v[72:75], v[132:135], v[32:47]\n\t" \
    "ds_read_b64_tr_b16 v[88:89], v106 offset:20480\n\t" \
    "ds_read_b64_tr_b16 v[90:91], v106 offset:21504\n\t" \
    "s_add_u32 %[soff], %[soff], 1024\n\t" \
    "s_waitcnt lgkmcnt(6)\n\t" \
    "v_mfma_f32_32x32x16_bf16 v[48:63], v[76:79], v[132:135], v[48:63]\n\t" \
    "ds_read_b64_tr_b16 v[92:93], v107 offset:20480\n\t" \
    "ds_read_b64_tr_b16 v[94:95], v107 offset:21504\n\t" \
    "s_waitcnt vmcnt(9) lgkmcnt(6)\n\t" \
    "v_mfma_f32_32x32x16_bf16 v[0:15], v[80:83], v[136:139], v[0:15]\n\t" \
    "ds_read_b64_tr_b16 v[64:65], v104 offset:24576\n\t" \
    "ds_read_b64_tr_b16 v[66:67], v104 offset:25600\n\t" \
    "s_waitcnt lgkmcnt(6)\n\t" \
    "v_mfma_f32_32x32x16_bf16 v[16:31], v[84:87], v[136:139], v[16:31]\n\t" \
    "ds_read_b64_tr_b16 v[68:69], v105 offset:24576\n\t" \
    "ds_read_b64_tr_b16 v[70:71], v105 offset:25600\n\t" \
    "buffer_load_dwordx4 v[128:131], v112, %[srd], %[soff] offen sc1\n\t" \
    "s_waitcnt lgkmcnt(6)\n\t" \
    "v_mfma_f32_32x32x16_bf16 v[32:47], v[88:91], v[136:139], v[32:47]\n\t" \
    "ds_read_b64_tr_b16 v[72:73], v106 offset:24576\n\t" \
    "ds_read_b64_tr_b16 v[74:75], v106 offset:25600\n\t" \
    "s_add_u32 %[soff], %[soff], 1024\n\t" \
    "s_waitcnt lgkmcnt(6)\n\t" \
    "v_mfma_f32_32x32x16_bf16 v[48:63], v[92:95], v[136:139], v[48:63]\n\t" \
    "ds_read_b64_tr_b16 v[76:77], v107 offset:24576\n\t" \
    "ds_read_b64_tr_b16 v[78:79], v107 offset:25600\n\t" \
    "s_waitcnt vmcnt(9) lgkmcnt(6)\n\t" \
    "v_mfma_f32_32x32x16_bf16 v[0:15], v[64:67], v[140:143], v[0:15]\n\t" \
    "ds_read_b64_tr_b16 v[80:81], v104 offset:28672\n\t" \
    "ds_read_b64_tr_b16 v[82:83], v104 offset:29696\n\t" \
    "s_waitcnt lgkmcnt(6)\n\t" \
    "v_mfma_f32_32x32x16_bf16 v[16:31], v[68:71], v[140:143], v[16:31]\n\t" \
    "ds_read_b64_tr_b16 v[84:85], v105 offset:28672\n\t" \
    "ds_read_b64_tr_b16 v[86:87], v105 offset:29696\n\t" \
    "buffer_load_dwordx4 v[132:135], v112, %[srd], %[soff] offen sc1\n\t" \
    "s_waitcnt lgkmcnt(6)\n\t" \
    "v_mfma_f32_32x32x16_bf16 v[32:47], v[72:75], v[140:143], v[32:47]\n\t" \
    "ds_read_b64_tr_b16 v[88:89], v106 offset:28672\n\t" \
    "ds_read_b64_tr_b16 v[90:91], v106 offset:29696\n\t" \
    "s_add_u32 %[soff], %[soff], 1024\n\t" \
    "s_waitcnt lgkmcnt(6)\n\t" \
    "v_mfma_f32_32x32x16_bf16 v[48:63], v[76:79], v[140:143], v[48:63]\n\t" \
    "ds_read_b64_tr_b16 v[92:93], v107 offset:28672\n\t" \
    "ds_read_b64_tr_b16 v[94:95], v107 offset:29696\n\t" \
    "s_waitcnt vmcnt(9) lgkmcnt(6)\n\t" \
    "v_mfma_f32_32x32x16_bf16 v[0:15], v[80:83], v[144:147], v[0:15]\n\t" \
    "s_waitcnt lgkmcnt(0)\n\t" \
    "s_barrier\n\t" \
    "ds_read_b64_tr_b16 v[64:65], v104 offset:32768\n\t" \
    "ds_read_b64_tr_b16 v[66:67], v104 offset:33792\n\t" \
    "s_waitcnt lgkmcnt(6)\n\t" \
    "v_mfma_f32_32x32x16_bf16 v[16:31], v[84:87], v[144:147], v[16:31]\n\t" \
    "ds_read_b64_tr_b16 v[68:69], v105 offset:32768\n\t" \
    "ds_read_b64_tr_b16 v[70:71], v105 offset:33792\n\t" \
    "buffer_load_dwordx4 v[136:139], v112, %[srd], %[soff] offen sc1\n\t" \
    "s_waitcnt lgkmcnt(6)\n\t" \
    "v_mfma_f32_32x32x16_bf16 v[32:47], v[88:91], v[144:147], v[32:47]\n\t" \
    "ds_read_b64_tr_b16 v[72:73], v106 offset:32768\n\t" \
    "ds_read_b64_tr_b16 v[74:75], v106 offset:33792\n\t" \
    "s_add_u32 %[soff], %[soff], 1024\n\t" \
    "s_waitcnt lgkmcnt(6)\n\t" \
    "v_mfma_f32_32x32x16_bf16 v[48:63], v[92:95], v[144:147], v[48:63]\n\t" \
    "ds_read_b64_tr_b16 v[76:77], v107 offset:32768\n\t" \
    "ds_read_b64_tr_b16 v[78:79], v107 offset:33792\n\t" \
    "s_waitcnt vmcnt(9) lgkmcnt(6)\n\t" \
    "v_mfma_f32_32x32x16_bf16 v[0:15], v[64:67], v[148:151], v[0:15]\n\t" \
    "ds_read_b64_tr_b16 v[80:81], v104 offset:36864\n\t" \
    "ds_read_b64_tr_b16 v[82:83], v104 offset:37888\n\t" \
    "s_waitcnt lgkmcnt(6)\n\t" \
    "v_mfma_f32_32x32x16_bf16 v[16:31], v[68:71], v[148:151], v[16:31]\n\t" \
    "ds_read_b64_tr_b16 v[84:85], v105 offset:36864\n\t" \
    "ds_read_b64_tr_b16 v[86:87], v105 offset:37888\n\t" \
    "buffer_load_dwordx4 v[140:143], v112, %[srd], %[soff] offen sc1\n\t" \
    "s_waitcnt lgkmcnt(6)\n\t" \
    "v_mfma_f32_32x32x16_bf16 v[32:47], v[72:75], v[148:151], v[32:47]\n\t" \
    "ds_read_b64_tr_b16 v[88:89], v106 offset:36864\n\t" \
    "ds_read_b64_tr_b16 v[90:91], v106 offset:37888\n\t" \
    "s_add_u32 %[soff], %[soff], 1024\n\t" \
    "s_waitcnt lgkmcnt(6)\n\t" \
    "v_mfma_f32_32x32x16_bf16 v[48:63], v[76:79], v[148:151], v[48:63]\n\t" \
    "ds_read_b64_tr_b16 v[92:93], v107 offset:36864\n\t" \
    "ds_read_b64_tr_b16 v[94:95], v107 offset:37888\n\t" \
    "s_waitcnt vmcnt(9) lgkmcnt(6)\n\t" \
    "v_mfma_f32_32x32x16_bf16 v[0:15], v[80:83], v[152:155], v[0:15]\n\t" \
    "ds_read_b64_tr_b16 v[64:65], v104 offset:40960\n\t" \
    "ds_read_b64_tr_b16 v[66:67], v104 offset:41984\n\t" \
    "s_waitcnt lgkmcnt(6)\n\t" \
    "v_mfma_f32_32x32x16_bf16 v[16:31], v[84:87], v[152:155], v[16:31]\n\t" \
    "ds_read_b64_tr_b16 v[68:69], v105 offset:40960\n\t" \
    "ds_read_b64_tr_b16 v[70:71], v105 offset:41984\n\t" \
    "buffer_load_dwordx4 v[144:147], v112, %[srd], %[soff] offen sc1\n\t" \
    "s_waitcnt lgkmcnt(6)\n\t" \
    "v_mfma_f32_32x32x16_bf16 v[32:47], v[88:91], v[152:155], v[32:47]\n\t" \
    "ds_read_b64_tr_b16 v[72:73], v106 offset:40960\n\t" \
    "ds_read_b64_tr_b16 v[74:75], v106 offset:41984\n\t" \
    "s_add_u32 %[soff], %[soff], 1024\n\t" \
    "s_waitcnt lgkmcnt(6)\n\t" \
    "v_mfma_f32_32x32x16_bf16 v[48:63], v[92:95], v[152:155], v[48:63]\n\t" \
    "ds_read_b64_tr_b16 v[76:77], v107 offset:40960\n\t" \
    "ds_read_b64_tr_b16 v[78:79], v107 offset:41984\n\t" \
    "s_waitcnt vmcnt(9) lgkmcnt(6)\n\t" \
    "v_mfma_f32_32x32x16_bf16 v[0:15], v[64:67], v[156:159], v[0:15]\n\t" \
    "ds_read_b64_tr_b16 v[80:81], v104 offset:45056\n\t" \
    "ds_read_b64_tr_b16 v[82:83], v104 offset:46080\n\t" \
    "s_waitcnt lgkmcnt(6)\n\t" \
    "v_mfma_f32_32x32x16_bf16 v[16:31], v[68:71], v[156:159], v[16:31]\n\t" \
    "ds_read_b64_tr_b16 v[84:85], v105 offset:45056\n\t" \
    "ds_read_b64_tr_b16 v[86:87], v105 offset:46080\n\t" \
    "buffer_load_dwordx4 v[148:151], v112, %[srd], %[soff] offen sc1\n\t" \
    "s_waitcnt lgkmcnt(6)\n\t" \
    "v_mfma_f32_32x32x16_bf16 v[32:47], v[72:75], v[156:159], v[32:47]\n\t" \
    "ds_read_b64_tr_b16 v[88:89], v106 offset:45056\n\t" \
    "ds_read_b64_tr_b16 v[90:91], v106 offset:46080\n\t" \
    "s_add_u32 %[soff], %[soff], 1024\n\t" \
    "s_waitcnt lgkmcnt(6)\n\t" \
    "v_mfma_f32_32x32x16_bf16 v[48:63], v[76:79], v[156:159], v[48:63]\n\t" \
    "ds_read_b64_tr_b16 v[92:93], v107 offset:45056\n\t" \
    "ds_read_b64_tr_b16 v[94:95], v107 offset:46080\n\t" \
    "s_waitcnt vmcnt(9) lgkmcnt(6)\n\t" \
    "v_mfma_f32_32x32x16_bf16 v[0:15], v[80:83], v[160:163], v[0:15]\n\t" \
    "s_waitcnt lgkmcnt(0)\n\t" \
    "s_barrier\n\t" \
    "ds_read_b64_tr_b16 v[64:65], v108 offset:0\n\t" \
    "ds_read_b64_tr_b16 v[66:67], v108 offset:1024\n\t" \
    "s_waitcnt lgkmcnt(6)\n\t" \
    "v_mfma_f32_32x32x16_bf16 v[16:31], v[84:87], v[160:163], v[16:31]\n\t" \
    "ds_read_b64_tr_b16 v[68:69], v109 offset:0\n\t" \
    "ds_read_b64_tr_b16 v[70:71], v109 offset:1024\n\t" \
    "buffer_load_dwordx4 v[152:155], v112, %[srd], %[soff] offen sc1\n\t" \
    "s_waitcnt lgkmcnt(6)\n\t" \
    "v_mfma_f32_32x32x16_bf16 v[32:47], v[88:91], v[160:163], v[32:47]\n\t" \
    "ds_read_b64_tr_b16 v[72:73], v110 offset:0\n\t" \
    "ds_read_b64_tr_b16 v[74:75], v110 offset:1024\n\t" \
    "s_add_u32 %[soff], %[soff], 1024\n\t" \
    "s_waitcnt lgkmcnt(6)\n\t" \
    "v_mfma_f32_32x32x16_bf16 v[48:63], v[92:95], v[160:163], v[48:63]\n\t" \
    "ds_read_b64_tr_b16 v[76:77], v111 offset:0\n\t" \
    "ds_read_b64_tr_b16 v[78:79], v111 offset:1024\n\t" \
    "s_waitcnt vmcnt(9) lgkmcnt(6)\n\t" \
    "v_mfma_f32_32x32x16_bf16 v[0:15], v[64:67], v[116:119], v[0:15]\n\t" \
    "ds_read_b64_tr_b16 v[80:81], v108 offset:4096\n\t" \
    "ds_read_b64_tr_b16 v[82:83], v108 offset:5120\n\t" \
    "s_waitcnt lgkmcnt(6)\n\t" \
    "v_mfma_f32_32x32x16_bf16 v[16:31], v[68:71], v[116:119], v[16:31]\n\t" \
    "ds_read_b64_tr_b16 v[84:85], v109 offset:4096\n\t" \
    "ds_read_b64_tr_b16 v[86:87], v109 offset:5120\n\t" \
    "buffer_load_dwordx4 v[156:159], v112, %[srd], %[soff] offen sc1\n\t" \
    "s_waitcnt lgkmcnt(6)\n\t" \
    "v_mfma_f32_32x32x16_bf16 v[32:47], v[72:75], v[116:119], v[32:47]\n\t" \
    "ds_read_b64_tr_b16 v[88:89], v110 offset:4096\n\t" \
    "ds_read_b64_tr_b16 v[90:91], v110 offset:5120\n\t" \
    "s_add_u32 %[soff], %[soff], 1024\n\t" \
    "s_waitcnt lgkmcnt(6)\n\t" \
    "v_mfma_f32_32x32x16_bf16 v[48:63], v[76:79], v[116:119], v[48:63]\n\t" \
    "ds_read_b64_tr_b16 v[92:93], v111 offset:4096\n\t" \
    "ds_read_b64_tr_b16 v[94:95], v111 offset:5120\n\t" \
    "s_waitcnt vmcnt(9) lgkmcnt(6)\n\t" \
    "v_mfma_f32_32x32x16_bf16 v[0:15], v[80:83], v[120:123], v[0:15]\n\t" \
    "ds_read_b64_tr_b16 v[64:65], v108 offset:8192\n\t" \
    "ds_read_b64_tr_b16 v[66:67], v108 offset:9216\n\t" \
    "s_waitcnt lgkmcnt(6)\n\t" \
    "v_mfma_f32_32x32x16_bf16 v[16:31], v[84:87], v[120:123], v[16:31]\n\t" \
    "ds_read_b64_tr_b16 v[68:69], v109 offset:8192\n\t" \
    "ds_read_b64_tr_b16 v[70:71], v109 offset:9216\n\t" \
    "buffer_load_dwordx4 v[160:163], v112, %[srd], %[soff] offen sc1\n\t" \
    "s_waitcnt lgkmcnt(6)\n\t" \
    "v_mfma_f32_32x32x16_bf16 v[32:47], v[88:91], v[120:123], v[32:47]\n\t" \
    "ds_read_b64_tr_b16 v[72:73], v110 offset:8192\n\t" \
    "ds_read_b64_tr_b16 v[74:75], v110 offset:9216\n\t" \
    "s_add_u32 %[soff], %[soff], 1024\n\t" \
    "s_waitcnt lgkmcnt(6)\n\t" \
    "v_mfma_f32_32x32x16_bf16 v[48:63], v[92:95], v[120:123], v[48:63]\n\t" \
    "ds_read_b64_tr_b16 v[76:77], v111 offset:8192\n\t" \
    "ds_read_b64_tr_b16 v[78:79], v111 offset:9216\n\t" \
    "s_waitcnt vmcnt(9) lgkmcnt(6)\n\t" \
    "v_mfma_f32_32x32x16_bf16 v[0:15], v[64:67], v[124:127], v[0:15]\n\t" \
    "ds_read_b64_tr_b16 v[80:81], v108 offset:12288\n\t" \
    "ds_read_b64_tr_b16 v[82:83], v108 offset:13312\n\t" \
    "s_waitcnt lgkmcnt(6)\n\t" \
    "v_mfma_f32_32x32x16_bf16 v[16:31], v[68:71], v[124:127], v[16:31]\n\t" \
    "ds_read_b64_tr_b16 v[84:85], v109 offset:12288\n\t" \
    "ds_read_b64_tr_b16 v[86:87], v109 offset:13312\n\t" \
    "buffer_load_dwordx4 v[116:119], v112, %[srd], %[soff] offen sc1\n\t" \
    "s_waitcnt lgkmcnt(6)\n\t" \
    "v_mfma_f32_32x32x16_bf16 v[32:47], v[72:75], v[124:127], v[32:47]\n\t" \
    "ds_read_b64_tr_b16 v[88:89], v110 offset:12288\n\t" \
    "ds_read_b64_tr_b16 v[90:91], v110 offset:13312\n\t" \
    "s_add_u32 %[soff], %[soff], 1024\n\t" \
    "s_waitcnt lgkmcnt(6)\n\t" \
    "v_mfma_f32_32x32x16_bf16 v[48:63], v[76:79], v[124:127], v[48:63]\n\t" \
    "ds_read_b64_tr_b16 v[92:93], v111 offset:12288\n\t" \
    "ds_read_b64_tr_b16 v[94:95], v111 offset:13312\n\t" \
    "s_waitcnt vmcnt(9) lgkmcnt(6)\n\t" \
    "v_mfma_f32_32x32x16_bf16 v[0:15], v[80:83], v[128:131], v[0:15]\n\t" \
    "s_waitcnt lgkmcnt(0)\n\t" \
    "s_barrier\n\t" \
    "ds_read_b64_tr_b16 v[64:65], v108 offset:16384\n\t" \
    "ds_read_b64_tr_b16 v[66:67], v108 offset:17408\n\t" \
    "s_waitcnt lgkmcnt(6)\n\t" \
    "v_mfma_f32_32x32x16_bf16 v[16:31], v[84:87], v[128:131], v[16:31]\n\t" \
    "ds_read_b64_tr_b16 v[68:69], v109 offset:16384\n\t" \
    "ds_read_b64_tr_b16 v[70:71], v109 offset:17408\n\t" \
    "buffer_load_dwordx4 v[120:123], v112, %[srd], %[soff] offen sc1\n\t" \
    "s_waitcnt lgkmcnt(6)\n\t" \
    "v_mfma_f32_32x32x16_bf16 v[32:47], v[88:91], v[128:131], v[32:47]\n\t" \
    "ds_read_b64_tr_b16 v[72:73], v110 offset:16384\n\t" \
    "ds_read_b64_tr_b16 v[74:75], v110 offset:17408\n\t" \
    "s_add_u32 %[soff], %[soff], 1024\n\t" \
    "s_waitcnt lgkmcnt(6)\n\t" \
    "v_mfma_f32_32x32x16_bf16 v[48:63], v[92:95], v[128:131], v[48:63]\n\t" \
    "ds_read_b64_tr_b16 v[76:77], v111 offset:16384\n\t" \
    "ds_read_b64_tr_b16 v[78:79], v111 offset:17408\n\t" \
    "s_waitcnt vmcnt(9) lgkmcnt(6)\n\t" \
    "v_mfma_f32_32x32x16_bf16 v[0:15], v[64:67], v[132:135], v[0:15]\n\t" \
    "ds_read_b64_tr_b16 v[80:81], v108 offset:20480\n\t" \
    "ds_read_b64_tr_b16 v[82:83], v108 offset:21504\n\t" \
    "s_waitcnt lgkmcnt(6)\n\t" \
    "v_mfma_f32_32x32x16_bf16 v[16:31], v[68:71], v[132:135], v[16:31]\n\t" \
    "ds_read_b64_tr_b16 v[84:85], v109 offset:20480\n\t" \
    "ds_read_b64_tr_b16 v[86:87], v109 offset:21504\n\t" \
    "buffer_load_dwordx4 v[124:127], v112, %[srd], %[soff] offen sc1\n\t" \
    "s_waitcnt lgkmcnt(6)\n\t" \
    "v_mfma_f32_32x32x16_bf16 v[32:47], v[72:75], v[132:135], v[32:47]\n\t" \
    "ds_read_b64_tr_b16 v[88:89], v110 offset:20480\n\t" \
    "ds_read_b64_tr_b16 v[90:91], v110 offset:21504\n\t" \
    "s_add_u32 %[soff], %[soff], 1024\n\t" \
    "s_waitcnt lgkmcnt(6)\n\t" \
    "v_mfma_f32_32x32x16_bf16 v[48:63], v[76:79], v[132:135], v[48:63]\n\t" \
    "ds_read_b64_tr_b16 v[92:93], v111 offset:20480\n\t" \
    "ds_read_b64_tr_b16 v[94:95], v111 offset:21504\n\t" \
    "s_waitcnt vmcnt(9) lgkmcnt(6)\n\t" \
    "v_mfma_f32_32x32x16_bf16 v[0:15], v[80:83], v[136:139], v[0:15]\n\t" \
    "ds_read_b64_tr_b16 v[64:65], v108 offset:24576\n\t" \
    "ds_read_b64_tr_b16 v[66:67], v108 offset:25600\n\t" \
    "s_waitcnt lgkmcnt(6)\n\t" \
    "v_mfma_f32_32x32x16_bf16 v[16:31], v[84:87], v[136:139], v[16:31]\n\t" \
    "ds_read_b64_tr_b16 v[68:69], v109 offset:24576\n\t" \
    "ds_read_b64_tr_b16 v[70:71], v109 offset:25600\n\t" \
    "buffer_load_dwordx4 v[128:131], v112, %[srd], %[soff] offen sc1\n\t" \
    "s_waitcnt lgkmcnt(6)\n\t" \
    "v_mfma_f32_32x32x16_bf16 v[32:47], v[88:91], v[136:139], v[32:47]\n\t" \
    "ds_read_b64_tr_b16 v[72:73], v110 offset:24576\n\t" \
    "ds_read_b64_tr_b16 v[74:75], v110 offset:25600\n\t" \
    "s_add_u32 %[soff], %[soff], 1024\n\t" \
    "s_waitcnt lgkmcnt(6)\n\t" \
    "v_mfma_f32_32x32x16_bf16 v[48:63], v[92:95], v[136:139], v[48:63]\n\t" \
    "ds_read_b64_tr_b16 v[76:77], v111 offset:24576\n\t" \
    "ds_read_b64_tr_b16 v[78:79], v111 offset:25600\n\t" \
    "s_waitcnt vmcnt(9) lgkmcnt(6)\n\t" \
    "v_mfma_f32_32x32x16_bf16 v[0:15], v[64:67], v[140:143], v[0:15]\n\t" \
    "ds_read_b64_tr_b16 v[80:81], v108 offset:28672\n\t" \
    "ds_read_b64_tr_b16 v[82:83], v108 offset:29696\n\t" \
    "s_waitcnt lgkmcnt(6)\n\t" \
    "v_mfma_f32_32x32x16_bf16 v[16:31], v[68:71], v[140:143], v[16:31]\n\t" \
    "ds_read_b64_tr_b16 v[84:85], v109 offset:28672\n\t" \
    "ds_read_b64_tr_b16 v[86:87], v109 offset:29696\n\t" \
    "buffer_load_dwordx4 v[132:135], v112, %[srd], %[soff] offen sc1\n\t" \
    "s_waitcnt lgkmcnt(6)\n\t" \
    "v_mfma_f32_32x32x16_bf16 v[32:47], v[72:75], v[140:143], v[32:47]\n\t" \
    "ds_read_b64_tr_b16 v[88:89], v110 offset:28672\n\t" \
    "ds_read_b64_tr_b16 v[90:91], v110 offset:29696\n\t" \
    "s_add_u32 %[soff], %[soff], 1024\n\t" \
    "s_waitcnt lgkmcnt(6)\n\t" \
    "v_mfma_f32_32x32x16_bf16 v[48:63], v[76:79], v[140:143], v[48:63]\n\t" \
    "ds_read_b64_tr_b16 v[92:93], v111 offset:28672\n\t" \
    "ds_read_b64_tr_b16 v[94:95], v111 offset:29696\n\t" \
    "s_waitcnt vmcnt(9) lgkmcnt(6)\n\t" \
    "v_mfma_f32_32x32x16_bf16 v[0:15], v[80:83], v[144:147], v[0:15]\n\t" \
    "s_waitcnt lgkmcnt(0)\n\t" \
    "s_barrier\n\t" \
    "ds_read_b64_tr_b16 v[64:65], v108 offset:32768\n\t" \
    "ds_read_b64_tr_b16 v[66:67], v108 offset:33792\n\t" \
    "s_waitcnt lgkmcnt(6)\n\t" \
    "v_mfma_f32_32x32x16_bf16 v[16:31], v[84:87], v[144:147], v[16:31]\n\t" \
    "ds_read_b64_tr_b16 v[68:69], v109 offset:32768\n\t" \
    "ds_read_b64_tr_b16 v[70:71], v109 offset:33792\n\t" \
    "buffer_load_dwordx4 v[136:139], v112, %[srd], %[soff] offen sc1\n\t" \
    "s_waitcnt lgkmcnt(6)\n\t" \
    "v_mfma_f32_32x32x16_bf16 v[32:47], v[88:91], v[144:147], v[32:47]\n\t" \
    "ds_read_b64_tr_b16 v[72:73], v110 offset:32768\n\t" \
    "ds_read_b64_tr_b16 v[74:75], v110 offset:33792\n\t" \
    "s_add_u32 %[soff], %[soff], 1024\n\t" \
    "s_waitcnt lgkmcnt(6)\n\t" \
    "v_mfma_f32_32x32x16_bf16 v[48:63], v[92:95], v[144:147], v[48:63]\n\t" \
    "ds_read_b64_tr_b16 v[76:77], v111 offset:32768\n\t" \
    "ds_read_b64_tr_b16 v[78:79], v111 offset:33792\n\t" \
    "s_waitcnt vmcnt(9) lgkmcnt(6)\n\t" \
    "v_mfma_f32_32x32x16_bf16 v[0:15], v[64:67], v[148:151], v[0:15]\n\t" \
    "ds_read_b64_tr_b16 v[80:81], v108 offset:36864\n\t" \
    "ds_read_b64_tr_b16 v[82:83], v108 offset:37888\n\t" \
    "s_waitcnt lgkmcnt(6)\n\t" \
    "v_mfma_f32_32x32x16_bf16 v[16:31], v[68:71], v[148:151], v[16:31]\n\t" \
    "ds_read_b64_tr_b16 v[84:85], v109 offset:36864\n\t" \
    "ds_read_b64_tr_b16 v[86:87], v109 offset:37888\n\t" \
    "buffer_load_dwordx4 v[140:143], v112, %[srd], %[soff] offen sc1\n\t" \
    "s_waitcnt lgkmcnt(6)\n\t" \
    "v_mfma_f32_32x32x16_bf16 v[32:47], v[72:75], v[148:151], v[32:47]\n\t" \
    "ds_read_b64_tr_b16 v[88:89], v110 offset:36864\n\t" \
    "ds_read_b64_tr_b16 v[90:91], v110 offset:37888\n\t" \
    "s_add_u32 %[soff], %[soff], 1024\n\t" \
    "s_waitcnt lgkmcnt(6)\n\t" \
    "v_mfma_f32_32x32x16_bf16 v[48:63], v[76:79], v[148:151], v[48:63]\n\t" \
    "ds_read_b64_tr_b16 v[92:93], v111 offset:36864\n\t" \
    "ds_read_b64_tr_b16 v[94:95], v111 offset:37888\n\t" \
    "s_waitcnt vmcnt(9) lgkmcnt(6)\n\t" \
    "v_mfma_f32_32x32x16_bf16 v[0:15], v[80:83], v[152:155], v[0:15]\n\t" \
    "ds_read_b64_tr_b16 v[64:65], v108 offset:40960\n\t" \
    "ds_read_b64_tr_b16 v[66:67], v108 offset:41984\n\t" \
    "s_waitcnt lgkmcnt(6)\n\t" \
    "v_mfma_f32_32x32x16_bf16 v[16:31], v[84:87], v[152:155], v[16:31]\n\t" \
    "ds_read_b64_tr_b16 v[68:69], v109 offset:40960\n\t" \
    "ds_read_b64_tr_b16 v[70:71], v109 offset:41984\n\t" \
    "buffer_load_dwordx4 v[144:147], v112, %[srd], %[soff] offen sc1\n\t" \
    "s_waitcnt lgkmcnt(6)\n\t" \
    "v_mfma_f32_32x32x16_bf16 v[32:47], v[88:91], v[152:155], v[32:47]\n\t" \
    "ds_read_b64_tr_b16 v[72:73], v110 offset:40960\n\t" \
    "ds_read_b64_tr_b16 v[74:75], v110 offset:41984\n\t" \
    "s_add_u32 %[soff], %[soff], 1024\n\t" \
    "s_waitcnt lgkmcnt(6)\n\t" \
    "v_mfma_f32_32x32x16_bf16 v[48:63], v[92:95], v[152:155], v[48:63]\n\t" \
    "ds_read_b64_tr_b16 v[76:77], v111 offset:40960\n\t" \
    "ds_read_b64_tr_b16 v[78:79], v111 offset:41984\n\t" \
    "s_waitcnt vmcnt(9) lgkmcnt(6)\n\t" \
    "v_mfma_f32_32x32x16_bf16 v[0:15], v[64:67], v[156:159], v[0:15]\n\t" \
    "ds_read_b64_tr_b16 v[80:81], v108 offset:45056\n\t" \
    "ds_read_b64_tr_b16 v[82:83], v108 offset:46080\n\t" \
    "s_waitcnt lgkmcnt(6)\n\t" \
    "v_mfma_f32_32x32x16_bf16 v[16:31], v[68:71], v[156:159], v[16:31]\n\t" \
    "ds_read_b64_tr_b16 v[84:85], v109 offset:45056\n\t" \
    "ds_read_b64_tr_b16 v[86:87], v109 offset:46080\n\t" \
    "buffer_load_dwordx4 v[148:151], v112, %[srd], %[soff] offen sc1\n\t" \
    "s_waitcnt lgkmcnt(6)\n\t" \
    "v_mfma_f32_32x32x16_bf16 v[32:47], v[72:75], v[156:159], v[32:47]\n\t" \
    "ds_read_b64_tr_b16 v[88:89], v110 offset:45056\n\t" \
    "ds_read_b64_tr_b16 v[90:91], v110 offset:46080\n\t" \
    "s_add_u32 %[soff], %[soff], 1024\n\t" \
    "s_waitcnt lgkmcnt(6)\n\t" \
    "v_mfma_f32_32x32x16_bf16 v[48:63], v[76:79], v[156:159], v[48:63]\n\t" \
    "ds_read_b64_tr_b16 v[92:93], v111 offset:45056\n\t" \
    "ds_read_b64_tr_b16 v[94:95], v111 offset:46080\n\t" \
    "s_waitcnt vmcnt(9) lgkmcnt(6)\n\t" \
    "v_mfma_f32_32x32x16_bf16 v[0:15], v[80:83], v[160:163], v[0:15]\n\t" \
    "s_waitcnt lgkmcnt(0)\n\t" \
    "s_barrier\n\t" \
    "ds_read_b64_tr_b16 v[64:65], v104 offset:0\n\t" \
    "ds_read_b64_tr_b16 v[66:67], v104 offset:1024\n\t" \
    "s_waitcnt lgkmcnt(6)\n\t" \
    "v_mfma_f32_32x32x16_bf16 v[16:31], v[84:87], v[160:163], v[16:31]\n\t" \
    "ds_read_b64_tr_b16 v[68:69], v105 offset:0\n\t" \
    "ds_read_b64_tr_b16 v[70:71], v105 offset:1024\n\t" \
    "buffer_load_dwordx4 v[152:155], v112, %[srd], %[soff] offen sc1\n\t" \
    "s_waitcnt lgkmcnt(6)\n\t" \
    "v_mfma_f32_32x32x16_bf16 v[32:47], v[88:91], v[160:163], v[32:47]\n\t" \
    "ds_read_b64_tr_b16 v[72:73], v106 offset:0\n\t" \
    "ds_read_b64_tr_b16 v[74:75], v106 offset:1024\n\t" \
    "s_add_u32 %[soff], %[soff], 1024\n\t" \
    "s_waitcnt lgkmcnt(6)\n\t" \
    "v_mfma_f32_32x32x16_bf16 v[48:63], v[92:95], v[160:163], v[48:63]\n\t" \
    "ds_read_b64_tr_b16 v[76:77], v107 offset:0\n\t" \
    "ds_read_b64_tr_b16 v[78:79], v107 offset:1024\n\t" \
    "s_waitcnt vmcnt(9) lgkmcnt(6)\n\t" \
    "v_mfma_f32_32x32x16_bf16 v[0:15], v[64:67], v[116:119], v[0:15]\n\t" \
    "ds_read_b64_tr_b16 v[80:81], v104 offset:4096\n\t" \
    "ds_read_b64_tr_b16 v[82:83], v104 offset:5120\n\t" \
    "s_waitcnt lgkmcnt(6)\n\t" \
    "v_mfma_f32_32x32x16_bf16 v[16:31], v[68:71], v[116:119], v[16:31]\n\t" \
    "ds_read_b64_tr_b16 v[84:85], v105 offset:4096\n\t" \
    "ds_read_b64_tr_b16 v[86:87], v105 offset:5120\n\t" \
    "buffer_load_dwordx4 v[156:159], v112, %[srd], %[soff] offen sc1\n\t" \
    "s_waitcnt lgkmcnt(6)\n\t" \
    "v_mfma_f32_32x32x16_bf16 v[32:47], v[72:75], v[116:119], v[32:47]\n\t" \
    "ds_read_b64_tr_b16 v[88:89], v106 offset:4096\n\t" \
    "ds_read_b64_tr_b16 v[90:91], v106 offset:5120\n\t" \
    "s_add_u32 %[soff], %[soff], 1024\n\t" \
    "s_waitcnt lgkmcnt(6)\n\t" \
    "v_mfma_f32_32x32x16_bf16 v[48:63], v[76:79], v[116:119], v[48:63]\n\t" \
    "ds_read_b64_tr_b16 v[92:93], v107 offset:4096\n\t" \
    "ds_read_b64_tr_b16 v[94:95], v107 offset:5120\n\t" \
    "s_waitcnt vmcnt(9) lgkmcnt(6)\n\t" \
    "v_mfma_f32_32x32x16_bf16 v[0:15], v[80:83], v[120:123], v[0:15]\n\t" \
    "ds_read_b64_tr_b16 v[64:65], v104 offset:8192\n\t" \
    "ds_read_b64_tr_b16 v[66:67], v104 offset:9216\n\t" \
    "s_waitcnt lgkmcnt(6)\n\t" \
    "v_mfma_f32_32x32x16_bf16 v[16:31], v[84:87], v[120:123], v[16:31]\n\t" \
    "ds_read_b64_tr_b16 v[68:69], v105 offset:8192\n\t" \
    "ds_read_b64_tr_b16 v[70:71], v105 offset:9216\n\t" \
    "buffer_load_dwordx4 v[160:163], v112, %[srd], %[soff] offen sc1\n\t" \
    "s_waitcnt lgkmcnt(6)\n\t" \
    "v_mfma_f32_32x32x16_bf16 v[32:47], v[88:91], v[120:123], v[32:47]\n\t" \
    "ds_read_b64_tr_b16 v[72:73], v106 offset:8192\n\t" \
    "ds_read_b64_tr_b16 v[74:75], v106 offset:9216\n\t" \
    "s_add_u32 %[soff], %[soff], 1024\n\t" \
    "s_waitcnt lgkmcnt(6)\n\t" \
    "v_mfma_f32_32x32x16_bf16 v[48:63], v[92:95], v[120:123], v[48:63]\n\t" \
    "ds_read_b64_tr_b16 v[76:77], v107 offset:8192\n\t" \
    "ds_read_b64_tr_b16 v[78:79], v107 offset:9216\n\t" \
    "s_waitcnt vmcnt(9) lgkmcnt(6)\n\t" \
    "v_mfma_f32_32x32x16_bf16 v[0:15], v[64:67], v[124:127], v[0:15]\n\t" \
    "ds_read_b64_tr_b16 v[80:81], v104 offset:12288\n\t" \
    "ds_read_b64_tr_b16 v[82:83], v104 offset:13312\n\t" \
    "s_waitcnt lgkmcnt(6)\n\t" \
    "v_mfma_f32_32x32x16_bf16 v[16:31], v[68:71], v[124:127], v[16:31]\n\t" \
    "ds_read_b64_tr_b16 v[84:85], v105 offset:12288\n\t" \
    "ds_read_b64_tr_b16 v[86:87], v105 offset:13312\n\t" \
    "buffer_load_dwordx4 v[116:119], v112, %[srd], %[soff] offen sc1\n\t" \
    "s_waitcnt lgkmcnt(6)\n\t" \
    "v_mfma_f32_32x32x16_bf16 v[32:47], v[72:75], v[124:127], v[32:47]\n\t" \
    "ds_read_b64_tr_b16 v[88:89], v106 offset:12288\n\t" \
    "ds_read_b64_tr_b16 v[90:91], v106 offset:13312\n\t" \
    "s_add_u32 %[soff], %[soff], 1024\n\t" \
    "s_waitcnt lgkmcnt(6)\n\t" \
    "v_mfma_f32_32x32x16_bf16 v[48:63], v[76:79], v[124:127], v[48:63]\n\t" \
    "ds_read_b64_tr_b16 v[92:93], v107 offset:12288\n\t" \
    "ds_read_b64_tr_b16 v[94:95], v107 offset:13312\n\t" \
    "s_waitcnt vmcnt(9) lgkmcnt(6)\n\t" \
    "v_mfma_f32_32x32x16_bf16 v[0:15], v[80:83], v[128:131], v[0:15]\n\t" \
    "s_waitcnt lgkmcnt(0)\n\t" \
    "s_barrier\n\t" \
    "ds_read_b64_tr_b16 v[64:65], v104 offset:16384\n\t" \
    "ds_read_b64_tr_b16 v[66:67], v104 offset:17408\n\t" \
    "s_waitcnt lgkmcnt(6)\n\t" \
    "v_mfma_f32_32x32x16_bf16 v[16:31], v[84:87], v[128:131], v[16:31]\n\t" \
    "ds_read_b64_tr_b16 v[68:69], v105 offset:16384\n\t" \
    "ds_read_b64_tr_b16 v[70:71], v105 offset:17408\n\t" \
    "buffer_load_dwordx4 v[120:123], v112, %[srd], %[soff] offen sc1\n\t" \
    "s_waitcnt lgkmcnt(6)\n\t" \
    "v_mfma_f32_32x32x16_bf16 v[32:47], v[88:91], v[128:131], v[32:47]\n\t" \
    "ds_read_b64_tr_b16 v[72:73], v106 offset:16384\n\t" \
    "ds_read_b64_tr_b16 v[74:75], v106 offset:17408\n\t" \
    "s_add_u32 %[soff], %[soff], 1024\n\t" \
    "s_waitcnt lgkmcnt(6)\n\t" \
    "v_mfma_f32_32x32x16_bf16 v[48:63], v[92:95], v[128:131], v[48:63]\n\t" \
    "ds_read_b64_tr_b16 v[76:77], v107 offset:16384\n\t" \
    "ds_read_b64_tr_b16 v[78:79], v107 offset:17408\n\t" \
    "s_waitcnt vmcnt(9) lgkmcnt(6)\n\t" \
    "v_mfma_f32_32x32x16_bf16 v[0:15], v[64:67], v[132:135], v[0:15]\n\t" \
    "ds_read_b64_tr_b16 v[80:81], v104 offset:20480\n\t" \
    "ds_read_b64_tr_b16 v[82:83], v104 offset:21504\n\t" \
    "s_waitcnt lgkmcnt(6)\n\t" \
    "v_mfma_f32_32x32x16_bf16 v[16:31], v[68:71], v[132:135], v[16:31]\n\t" \
    "ds_read_b64_tr_b16 v[84:85], v105 offset:20480\n\t" \
    "ds_read_b64_tr_b16 v[86:87], v105 offset:21504\n\t" \
    "buffer_load_dwordx4 v[124:127], v112, %[srd], %[soff] offen sc1\n\t" \
    "s_waitcnt lgkmcnt(6)\n\t" \
    "v_mfma_f32_32x32x16_bf16 v[32:47], v[72:75], v[132:135], v[32:47]\n\t" \
    "ds_read_b64_tr_b16 v[88:89], v106 offset:20480\n\t" \
    "ds_read_b64_tr_b16 v[90:91], v106 offset:21504\n\t" \
    "s_add_u32 %[soff], %[soff], 1024\n\t" \
    "s_waitcnt lgkmcnt(6)\n\t" \
    "v_mfma_f32_32x32x16_bf16 v[48:63], v[76:79], v[132:135], v[48:63]\n\t" \
    "ds_read_b64_tr_b16 v[92:93], v107 offset:20480\n\t" \
    "ds_read_b64_tr_b16 v[94:95], v107 offset:21504\n\t" \
    "s_waitcnt vmcnt(9) lgkmcnt(6)\n\t" \
    "v_mfma_f32_32x32x16_bf16 v[0:15], v[80:83], v[136:139], v[0:15]\n\t" \
    "ds_read_b64_tr_b16 v[64:65], v104 offset:24576\n\t" \
    "ds_read_b64_tr_b16 v[66:67], v104 offset:25600\n\t" \
    "s_waitcnt lgkmcnt(6)\n\t" \
    "v_mfma_f32_32x32x16_bf16 v[16:31], v[84:87], v[136:139], v[16:31]\n\t" \
    "ds_read_b64_tr_b16 v[68:69], v105 offset:24576\n\t" \
    "ds_read_b64_tr_b16 v[70:71], v105 offset:25600\n\t" \
    "buffer_load_dwordx4 v[128:131], v112, %[srd], %[soff] offen sc1\n\t" \
    "s_waitcnt lgkmcnt(6)\n\t" \
    "v_mfma_f32_32x32x16_bf16 v[32:47], v[88:91], v[136:139], v[32:47]\n\t" \
    "ds_read_b64_tr_b16 v[72:73], v106 offset:24576\n\t" \
    "ds_read_b64_tr_b16 v[74:75], v106 offset:25600\n\t" \
    "s_add_u32 %[soff], %[soff], 1024\n\t" \
    "s_waitcnt lgkmcnt(6)\n\t" \
    "v_mfma_f32_32x32x16_bf16 v[48:63], v[92:95], v[136:139], v[48:63]\n\t" \
    "ds_read_b64_tr_b16 v[76:77], v107 offset:24576\n\t" \
    "ds_read_b64_tr_b16 v[78:79], v107 offset:25600\n\t" \
    "s_waitcnt vmcnt(9) lgkmcnt(6)\n\t" \
    "v_mfma_f32_32x32x16_bf16 v[0:15], v[64:67], v[140:143], v[0:15]\n\t" \
    "ds_read_b64_tr_b16 v[80:81], v104 offset:28672\n\t" \
    "ds_read_b64_tr_b16 v[82:83], v104 offset:29696\n\t" \
    "s_waitcnt lgkmcnt(6)\n\t" \
    "v_mfma_f32_32x32x16_bf16 v[16:31], v[68:71], v[140:143], v[16:31]\n\t" \
    "ds_read_b64_tr_b16 v[84:85], v105 offset:28672\n\t" \
    "ds_read_b64_tr_b16 v[86:87], v105 offset:29696\n\t" \
    "s_waitcnt lgkmcnt(6)\n\t" \
    "v_mfma_f32_32x32x16_bf16 v[32:47], v[72:75], v[140:143], v[32:47]\n\t" \
    "ds_read_b64_tr_b16 v[88:89], v106 offset:28672\n\t" \
    "ds_read_b64_tr_b16 v[90:91], v106 offset:29696\n\t" \
    "s_waitcnt lgkmcnt(6)\n\t" \
    "v_mfma_f32_32x32x16_bf16 v[48:63], v[76:79], v[140:143], v[48:63]\n\t" \
    "ds_read_b64_tr_b16 v[92:93], v107 offset:28672\n\t" \
    "ds_read_b64_tr_b16 v[94:95], v107 offset:29696\n\t" \
    "s_waitcnt vmcnt(8) lgkmcnt(6)\n\t" \
    "v_mfma_f32_32x32x16_bf16 v[0:15], v[80:83], v[144:147], v[0:15]\n\t" \
    "s_waitcnt lgkmcnt(0)\n\t" \
    "s_barrier\n\t" \
    "ds_read_b64_tr_b16 v[64:65], v104 offset:32768\n\t" \
    "ds_read_b64_tr_b16 v[66:67], v104 offset:33792\n\t" \
    "s_waitcnt lgkmcnt(6)\n\t" \
    "v_mfma_f32_32x32x16_bf16 v[16:31], v[84:87], v[144:147], v[16:31]\n\t" \
    "ds_read_b64_tr_b16 v[68:69], v105 offset:32768\n\t" \
    "ds_read_b64_tr_b16 v[70:71], v105 offset:33792\n\t" \
    "s_waitcnt lgkmcnt(6)\n\t" \
    "v_mfma_f32_32x32x16_bf16 v[32:47], v[88:91], v[144:147], v[32:47]\n\t" \
    "ds_read_b64_tr_b16 v[72:73], v106 offset:32768\n\t" \
    "ds_read_b64_tr_b16 v[74:75], v106 offset:33792\n\t" \
    "s_waitcnt lgkmcnt(6)\n\t" \
    "v_mfma_f32_32x32x16_bf16 v[48:63], v[92:95], v[144:147], v[48:63]\n\t" \
    "ds_read_b64_tr_b16 v[76:77], v107 offset:32768\n\t" \
    "ds_read_b64_tr_b16 v[78:79], v107 offset:33792\n\t" \
    "s_waitcnt vmcnt(7) lgkmcnt(6)\n\t" \
    "v_mfma_f32_32x32x16_bf16 v[0:15], v[64:67], v[148:151], v[0:15]\n\t" \
    "ds_read_b64_tr_b16 v[80:81], v104 offset:36864\n\t" \
    "ds_read_b64_tr_b16 v[82:83], v104 offset:37888\n\t" \
    "s_waitcnt lgkmcnt(6)\n\t" \
    "v_mfma_f32_32x32x16_bf16 v[16:31], v[68:71], v[148:151], v[16:31]\n\t" \
    "ds_read_b64_tr_b16 v[84:85], v105 offset:36864\n\t" \
    "ds_read_b64_tr_b16 v[86:87], v105 offset:37888\n\t" \
    "s_waitcnt lgkmcnt(6)\n\t" \
    "v_mfma_f32_32x32x16_bf16 v[32:47], v[72:75], v[148:151], v[32:47]\n\t" \
    "ds_read_b64_tr_b16 v[88:89], v106 offset:36864\n\t" \
    "ds_read_b64_tr_b16 v[90:91], v106 offset:37888\n\t" \
    "s_waitcnt lgkmcnt(6)\n\t" \
    "v_mfma_f32_32x32x16_bf16 v[48:63], v[76:79], v[148:151], v[48:63]\n\t" \
    "ds_read_b64_tr_b16 v[92:93], v107 offset:36864\n\t" \
    "ds_read_b64_tr_b16 v[94:95], v107 offset:37888\n\t" \
    "s_waitcnt vmcnt(6) lgkmcnt(6)\n\t" \
    "v_mfma_f32_32x32x16_bf16 v[0:15], v[80:83], v[152:155], v[0:15]\n\t" \
    "ds_read_b64_tr_b16 v[64:65], v104 offset:40960\n\t" \
    "ds_read_b64_tr_b16 v[66:67], v104 offset:41984\n\t" \
    "s_waitcnt lgkmcnt(6)\n\t" \
    "v_mfma_f32_32x32x16_bf16 v[16:31], v[84:87], v[152:155], v[16:31]\n\t" \
    "ds_read_b64_tr_b16 v[68:69], v105 offset:40960\n\t" \
    "ds_read_b64_tr_b16 v[70:71], v105 offset:41984\n\t" \
    "s_waitcnt lgkmcnt(6)\n\t" \
    "v_mfma_f32_32x32x16_bf16 v[32:47], v[88:91], v[152:155], v[32:47]\n\t" \
    "ds_read_b64_tr_b16 v[72:73], v106 offset:40960\n\t" \
    "ds_read_b64_tr_b16 v[74:75], v106 offset:41984\n\t" \
    "s_waitcnt lgkmcnt(6)\n\t" \
    "v_mfma_f32_32x32x16_bf16 v[48:63], v[92:95], v[152:155], v[48:63]\n\t" \
    "ds_read_b64_tr_b16 v[76:77], v107 offset:40960\n\t" \
    "ds_read_b64_tr_b16 v[78:79], v107 offset:41984\n\t" \
    "s_waitcnt vmcnt(5) lgkmcnt(6)\n\t" \
    "v_mfma_f32_32x32x16_bf16 v[0:15], v[64:67], v[156:159], v[0:15]\n\t" \
    "ds_read_b64_tr_b16 v[80:81], v104 offset:45056\n\t" \
    "ds_read_b64_tr_b16 v[82:83], v104 offset:46080\n\t" \
    "s_waitcnt lgkmcnt(6)\n\t" \
    "v_mfma_f32_32x32x16_bf16 v[16:31], v[68:71], v[156:159], v[16:31]\n\t" \
    "ds_read_b64_tr_b16 v[84:85], v105 offset:45056\n\t" \
    "ds_read_b64_tr_b16 v[86:87], v105 offset:46080\n\t" \
    "s_waitcnt lgkmcnt(6)\n\t" \
    "v_mfma_f32_32x32x16_bf16 v[32:47], v[72:75], v[156:159], v[32:47]\n\t" \
    "ds_read_b64_tr_b16 v[88:89], v106 offset:45056\n\t" \
    "ds_read_b64_tr_b16 v[90:91], v106 offset:46080\n\t" \
    "s_waitcnt lgkmcnt(6)\n\t" \
    "v_mfma_f32_32x32x16_bf16 v[48:63], v[76:79], v[156:159], v[48:63]\n\t" \
    "ds_read_b64_tr_b16 v[92:93], v107 offset:45056\n\t" \
    "ds_read_b64_tr_b16 v[94:95], v107 offset:46080\n\t" \
    "s_waitcnt vmcnt(4) lgkmcnt(6)\n\t" \
    "v_mfma_f32_32x32x16_bf16 v[0:15], v[80:83], v[160:163], v[0:15]\n\t" \
    "s_waitcnt lgkmcnt(0)\n\t" \
    "s_barrier\n\t" \
    "ds_read_b64_tr_b16 v[64:65], v108 offset:0\n\t" \
    "ds_read_b64_tr_b16 v[66:67], v108 offset:1024\n\t" \
    "s_waitcnt lgkmcnt(6)\n\t" \
    "v_mfma_f32_32x32x16_bf16 v[16:31], v[84:87], v[160:163], v[16:31]\n\t" \
    "ds_read_b64_tr_b16 v[68:69], v109 offset:0\n\t" \
    "ds_read_b64_tr_b16 v[70:71], v109 offset:1024\n\t" \
    "s_waitcnt lgkmcnt(6)\n\t" \
    "v_mfma_f32_32x32x16_bf16 v[32:47], v[88:91], v[160:163], v[32:47]\n\t" \
    "ds_read_b64_tr_b16 v[72:73], v110 offset:0\n\t" \
    "ds_read_b64_tr_b16 v[74:75], v110 offset:1024\n\t" \
    "s_waitcnt lgkmcnt(6)\n\t" \
    "v_mfma_f32_32x32x16_bf16 v[48:63], v[92:95], v[160:163], v[48:63]\n\t" \
    "ds_read_b64_tr_b16 v[76:77], v111 offset:0\n\t" \
    "ds_read_b64_tr_b16 v[78:79], v111 offset:1024\n\t" \
    "s_waitcnt vmcnt(3) lgkmcnt(6)\n\t" \
    "v_mfma_f32_32x32x16_bf16 v[0:15], v[64:67], v[116:119], v[0:15]\n\t" \
    "ds_read_b64_tr_b16 v[80:81], v108 offset:4096\n\t" \
    "ds_read_b64_tr_b16 v[82:83], v108 offset:5120\n\t" \
    "s_waitcnt lgkmcnt(6)\n\t" \
    "v_mfma_f32_32x32x16_bf16 v[16:31], v[68:71], v[116:119], v[16:31]\n\t" \
    "ds_read_b64_tr_b16 v[84:85], v109 offset:4096\n\t" \
    "ds_read_b64_tr_b16 v[86:87], v109 offset:5120\n\t" \
    "s_waitcnt lgkmcnt(6)\n\t" \
    "v_mfma_f32_32x32x16_bf16 v[32:47], v[72:75], v[116:119], v[32:47]\n\t" \
    "ds_read_b64_tr_b16 v[88:89], v110 offset:4096\n\t" \
    "ds_read_b64_tr_b16 v[90:91], v110 offset:5120\n\t" \
    "s_waitcnt lgkmcnt(6)\n\t" \
    "v_mfma_f32_32x32x16_bf16 v[48:63], v[76:79], v[116:119], v[48:63]\n\t" \
    "ds_read_b64_tr_b16 v[92:93], v111 offset:4096\n\t" \
    "ds_read_b64_tr_b16 v[94:95], v111 offset:5120\n\t" \
    "s_waitcnt vmcnt(2) lgkmcnt(6)\n\t" \
    "v_mfma_f32_32x32x16_bf16 v[0:15], v[80:83], v[120:123], v[0:15]\n\t" \
    "ds_read_b64_tr_b16 v[64:65], v108 offset:8192\n\t" \
    "ds_read_b64_tr_b16 v[66:67], v108 offset:9216\n\t" \
    "s_waitcnt lgkmcnt(6)\n\t" \
    "v_mfma_f32_32x32x16_bf16 v[16:31], v[84:87], v[120:123], v[16:31]\n\t" \
    "ds_read_b64_tr_b16 v[68:69], v109 offset:8192\n\t" \
    "ds_read_b64_tr_b16 v[70:71], v109 offset:9216\n\t" \
    "s_waitcnt lgkmcnt(6)\n\t" \
    "v_mfma_f32_32x32x16_bf16 v[32:47], v[88:91], v[120:123], v[32:47]\n\t" \
    "ds_read_b64_tr_b16 v[72:73], v110 offset:8192\n\t" \
    "ds_read_b64_tr_b16 v[74:75], v110 offset:9216\n\t" \
    "s_waitcnt lgkmcnt(6)\n\t" \
    "v_mfma_f32_32x32x16_bf16 v[48:63], v[92:95], v[120:123], v[48:63]\n\t" \
    "ds_read_b64_tr_b16 v[76:77], v111 offset:8192\n\t" \
    "ds_read_b64_tr_b16 v[78:79], v111 offset:9216\n\t" \
    "s_waitcnt vmcnt(1) lgkmcnt(6)\n\t" \
    "v_mfma_f32_32x32x16_bf16 v[0:15], v[64:67], v[124:127], v[0:15]\n\t" \
    "ds_read_b64_tr_b16 v[80:81], v108 offset:12288\n\t" \
    "ds_read_b64_tr_b16 v[82:83], v108 offset:13312\n\t" \
    "s_waitcnt lgkmcnt(6)\n\t" \
    "v_mfma_f32_32x32x16_bf16 v[16:31], v[68:71], v[124:127], v[16:31]\n\t" \
    "ds_read_b64_tr_b16 v[84:85], v109 offset:12288\n\t" \
    "ds_read_b64_tr_b16 v[86:87], v109 offset:13312\n\t" \
    "s_waitcnt lgkmcnt(6)\n\t" \
    "v_mfma_f32_32x32x16_bf16 v[32:47], v[72:75], v[124:127], v[32:47]\n\t" \
    "ds_read_b64_tr_b16 v[88:89], v110 offset:12288\n\t" \
    "ds_read_b64_tr_b16 v[90:91], v110 offset:13312\n\t" \
    "s_waitcnt lgkmcnt(6)\n\t" \
    "v_mfma_f32_32x32x16_bf16 v[48:63], v[76:79], v[124:127], v[48:63]\n\t" \
    "ds_read_b64_tr_b16 v[92:93], v111 offset:12288\n\t" \
    "ds_read_b64_tr_b16 v[94:95], v111 offset:13312\n\t" \
    "s_waitcnt vmcnt(0) lgkmcnt(6)\n\t" \
    "v_mfma_f32_32x32x16_bf16 v[0:15], v[80:83], v[128:131], v[0:15]\n\t" \
    "s_waitcnt lgkmcnt(4)\n\t" \
    "v_mfma_f32_32x32x16_bf16 v[16:31], v[84:87], v[128:131], v[16:31]\n\t" \
    "s_waitcnt lgkmcnt(2)\n\t" \
    "v_mfma_f32_32x32x16_bf16 v[32:47], v[88:91], v[128:131], v[32:47]\n\t" \
    "s_waitcnt lgkmcnt(0)\n\t" \
    "v_mfma_f32_32x32x16_bf16 v[48:63], v[92:95], v[128:131], v[48:63]\n\t" \
    "s_nop 15\n\t"


constexpr unsigned WS_CNT = 0;
constexpr unsigned WS_Q = 16384;
constexpr unsigned WS_KV = WS_Q + 32 * 80 * 1024;
constexpr unsigned WS_KN = WS_KV + 32 * 16 * 10240;
constexpr unsigned WS_CS = WS_KN + 4096;
constexpr unsigned WS_O = WS_CS + 8192;
constexpr unsigned WS_WO = WS_O + 128 * 80 * 256;
constexpr unsigned WS_W2 = WS_WO + 40 * 20 * 1024;
constexpr unsigned WS_H = WS_W2 + 40 * 54 * 1024;
constexpr unsigned WS_ST = WS_H + 2048 * 640 * 2;
constexpr unsigned WS_HID = WS_ST + 2048 * 16 * 8;
constexpr unsigned WS_END = WS_HID + 128 * 216 * 256;

constexpr float QSCALE = 0.22360679774997896f * 1.4426950408889634f;
constexpr float LN_EPS = 1e-5f;

DEV unsigned cvtpk(float lo, float hi) { f32x2 v = {lo, hi}; return __builtin_bit_cast(unsigned, __builtin_convertvector(v, bf16x2_t)); }
DEV float bflo(unsigned u) { return __uint_as_float(u << 16); }
DEV float bfhi(unsigned u) { return __uint_as_float(u & 0xffff0000u); }
DEV f32x16 mfma32(u32x4 a, u32x4 b, f32x16 c) { return __builtin_amdgcn_mfma_f32_32x32x16_bf16(__builtin_bit_cast(bf16x8_t, a), __builtin_bit_cast(bf16x8_t, b), c, 0, 0, 0); }
DEV f32x16 mfma32k8(u32x2 a, u32x2 b, f32x16 c) { return __builtin_amdgcn_mfma_f32_32x32x8bf16_1k(__builtin_bit_cast(s16x4, a), __builtin_bit_cast(s16x4, b), c, 0, 0, 0); }
DEV f32x4 mfma16(u32x4 a, u32x4 b, f32x4 c) { return __builtin_amdgcn_mfma_f32_16x16x32_bf16(__builtin_bit_cast(bf16x8_t, a), __builtin_bit_cast(bf16x8_t, b), c, 0, 0, 0); }
DEV float swap32_add(float v) { auto r = __builtin_amdgcn_permlane32_swap(__float_as_uint(v), __float_as_uint(v), false, false); return __uint_as_float(r[0]) + __uint_as_float(r[1]); }
DEV float swap32_max(float v) { auto r = __builtin_amdgcn_permlane32_swap(__float_as_uint(v), __float_as_uint(v), false, false); return fmaxf(__uint_as_float(r[0]), __uint_as_float(r[1])); }
DEV float other32(float v, int hi) { auto r = __builtin_amdgcn_permlane32_swap(__float_as_uint(v), __float_as_uint(v), false, false); return __uint_as_float(hi ? r[0] : r[1]); }
DEV unsigned wave_max63(unsigned v) {
    v = max(v, (unsigned)__builtin_amdgcn_update_dpp(0, (int)v, 0x111, 0xf, 0xf, true));
    v = max(v, (unsigned)__builtin_amdgcn_update_dpp(0, (int)v, 0x112, 0xf, 0xf, true));
    v = max(v, (unsigned)__builtin_amdgcn_update_dpp(0, (int)v, 0x114, 0xf, 0xf, true));
    v = max(v, (unsigned)__builtin_amdgcn_update_dpp(0, (int)v, 0x118, 0xf, 0xf, true));
    v = max(v, (unsigned)__builtin_amdgcn_update_dpp(0, (int)v, 0x142, 0xa, 0xf, true));
    v = max(v, (unsigned)__builtin_amdgcn_update_dpp(0, (int)v, 0x143, 0xc, 0xf, true));
    return v;
}
DEV float sumsq2(unsigned u, float c) { const float a = bflo(u), b = bfhi(u); return fmaf(a, a, fmaf(b, b, c)); }
DEV void glds16(unsigned voff, const void* sbase, unsigned ldsaddr) {
    unsigned sv;
    asm volatile("s_mov_b32 %0, m0\n\ts_mov_b32 m0, %3\n\ts_nop 0\n\tglobal_load_lds_dwordx4 %1, %2\n\ts_mov_b32 m0, %0"
                 : "=&s"(sv) : "v"(voff), "s"(sbase), "s"(ldsaddr) : "memory");
}
#define WAITV(n) asm volatile("s_waitcnt vmcnt(" #n ")" ::: "memory")
#define WAITL0() asm volatile("s_waitcnt lgkmcnt(0)" ::: "memory")
#define BAR() do { asm volatile("" ::: "memory"); __builtin_amdgcn_s_barrier(); asm volatile("" ::: "memory"); } while (0)
DEV u32x4 lds128(LAS const char* p) { return *(LAS const u32x4*)p; }
DEV u32x2 lds64(LAS const char* p) { return *(LAS const u32x2*)p; }

constexpr int G_STAGE = 32768, G_RING = 4 * G_STAGE;
constexpr int G_STAT = G_RING;
constexpr int G_CS = G_STAT + 1024;
constexpr int G_LDS = G_CS + 512;

#define GAS __attribute__((address_space(1)))
typedef __amdgpu_buffer_rsrc_t rsrc_t;
DEV u32x4 ld128_dev(rsrc_t r, unsigned voff, unsigned soff) { return __builtin_amdgcn_raw_buffer_load_b128(r, voff, soff, 16); }
DEV u32x2 ld64_dev(rsrc_t r, unsigned voff, unsigned soff) { return __builtin_amdgcn_raw_buffer_load_b64(r, voff, soff, 16); }
DEV void st128_dev(u32x4 v, rsrc_t r, unsigned voff, unsigned soff) { __builtin_amdgcn_raw_buffer_store_b128(v, r, voff + soff, 0, 16); }
DEV void st64_dev(u32x2 v, rsrc_t r, unsigned voff, unsigned soff) { __builtin_amdgcn_raw_buffer_store_b64(v, r, voff, soff, 16); }
constexpr int NPREP = 2960;
DEV void prep_addr(int task, int lane, unsigned& o0, unsigned& o1, unsigned& dst) {
    const int col = lane & 15, g = lane >> 4;
    if (task < 800) {
        const int ct = task % 40, ks = task / 40;
        const int q = 4 * ks + g, p = (q * 205) >> 10, c = q - 5 * p;
        const int k0 = c < 4 ? (2 * p + (c >> 1)) * 20 + 4 * (c & 1) : 40 * p + 16, k1 = c < 4 ? k0 + 8 : k0 + 20;
        o0 = (unsigned)(k0 * 1280 + (ct * 16 + col) * 2); o1 = (unsigned)(k1 * 1280 + (ct * 16 + col) * 2);
        dst = (unsigned)(WS_WO + (ct * 20 + ks) * 1024 + lane * 16);
    } else {
        const int t = task - 800, ct = t % 40, ks = t / 40;
        const int k0 = 32 * ks + 16 * (g >> 1) + 4 * (g & 1);
        o0 = (unsigned)(k0 * 1280 + (ct * 16 + col) * 2); o1 = o0 + 8 * 1280;
        dst = (unsigned)(WS_W2 + (ks < 28 ? ct * 28 + ks : 40 * 28 + ct * 26 + ks - 28) * 1024 + lane * 16);
    }
}
struct PrepRegs { unsigned e[8]; unsigned dst; };
DEV void prep_gather(int task, int lane, const char* wo, const char* w2, PrepRegs& p) {
    unsigned o0, o1;
    prep_addr(task, lane, o0, o1, p.dst);
    const char* base = task < 800 ? wo : w2;
    asm volatile("global_load_ushort %0, %8, %10\n\tglobal_load_ushort %1, %8, %10 offset:1280\n\t"
                 "global_load_ushort %2, %8, %10 offset:2560\n\tglobal_load_ushort %3, %8, %10 offset:3840\n\t"
                 "global_load_ushort %4, %9, %10\n\tglobal_load_ushort %5, %9, %10 offset:1280\n\t"
                 "global_load_ushort %6, %9, %10 offset:2560\n\tglobal_load_ushort %7, %9, %10 offset:3840"
                 : "=&v"(p.e[0]), "=&v"(p.e[1]), "=&v"(p.e[2]), "=&v"(p.e[3]), "=&v"(p.e[4]), "=&v"(p.e[5]), "=&v"(p.e[6]), "=&v"(p.e[7])
                 : "v"(o0), "v"(o1), "s"(base) : "memory");
}
#define PREP_LANDED(p) asm volatile("" : "+v"((p).e[0]), "+v"((p).e[1]), "+v"((p).e[2]), "+v"((p).e[3]), "+v"((p).e[4]), "+v"((p).e[5]), "+v"((p).e[6]), "+v"((p).e[7]))
DEV void k1_st128(u32x4 v, char* ws, unsigned voff, unsigned soff) {
    __builtin_amdgcn_raw_buffer_store_b128(v, __builtin_amdgcn_make_buffer_rsrc(ws, 0, WS_END, 0x00020000), voff + soff, 0, 16);
}
DEV void prep_store(const PrepRegs& p, char* ws) {
    k1_st128((u32x4){p.e[0] | (p.e[1] << 16), p.e[2] | (p.e[3] << 16), p.e[4] | (p.e[5] << 16), p.e[6] | (p.e[7] << 16)}, ws, p.dst, 0);
}

DEV void glds16n(unsigned voff, const void* sbase, unsigned ldsaddr) {
    asm volatile("s_mov_b32 m0, %2\n\ts_nop 0\n\tglobal_load_lds_dwordx4 %0, %1" :: "v"(voff), "s"(sbase), "s"(ldsaddr) : "memory");
}
DEV void g_loader(char* ws_, const char* xb, const char* wb, int l, int lane, int wcol  ,
                  const char* wo_, const char* w2_, int task0, bool keyblock) {
    constexpr int WROW = 3840;
    const int r8 = lane >> 3, pos = lane & 7;
    const unsigned xo0 = (unsigned)(r8 * 1280 + ((pos ^ ((lane >> 4) & 7)) * 16));
    const unsigned xo1 = (unsigned)(r8 * 1280 + ((pos ^ (4 + (lane >> 4))) * 16));
    const int r4 = lane >> 4, p16 = lane & 15;
    const unsigned wo = (unsigned)(r4 * WROW + wcol + ((p16 ^ (r4 << 2)) * 16));
    const char* xs = xb + (size_t)l * 32 * 1280;
    const char* ws = wb + (size_t)l * 16 * WROW;
    auto issue = [&](int s) {
        const unsigned dst = (unsigned)(s & 3) * G_STAGE + (unsigned)l * 4096;
        const char* xp = xs + s * 128;
        const char* wp = ws + (size_t)s * 64 * WROW;
#pragma unroll
        for (int i = 0; i < 4; ++i) { glds16n((i & 1) ? xo1 : xo0, xp + i * 8 * 1280, dst + i * 1024); glds16n(wo, wp + (size_t)i * 4 * WROW, dst + 16384 + i * 1024); }
    };

    issue(0); issue(1);

    WAITV(8); BAR();
    issue(2); issue(3);
#pragma unroll 1
    for (int s = 1; s < 7; ++s) { WAITV(16); BAR(); issue(s + 3); }
    PrepRegs p0, p1, p2;
    WAITV(16); BAR();
    prep_gather(task0, lane, wo_, w2_, p0);
    WAITV(16); BAR();
    prep_gather(task0 + 1, lane, wo_, w2_, p1);
    WAITV(16); BAR();
    prep_gather(task0 + 2, lane, wo_, w2_, p2);
    BAR();
    BAR();
    WAITV(0);
    PREP_LANDED(p0); PREP_LANDED(p1); PREP_LANDED(p2);
    prep_store(p0, ws_); prep_store(p1, ws_); prep_store(p2, ws_);
    if (keyblock) BAR();
}

__global__ __launch_bounds__(512, 2) void k_qkv(const char* __restrict__ xin, const char* __restrict__ win, const char* __restrict__ w1,
                                                const char* __restrict__ wo, const char* __restrict__ w2, char* __restrict__ ws) {
    extern __shared__ __attribute__((aligned(16))) char smem[];
    const int b = blockIdx.x, xcd = b & 7, i = b >> 3;
    const int rb = (xcd & 1) * 8 + (i & 7), cb = (xcd >> 1) + 4 * (i >> 3);
    const int tid = threadIdx.x, lane = tid & 63, wid = __builtin_amdgcn_readfirstlane(tid >> 6);
    if (b < 49 && tid < 16) *(u32x4*)(ws + WS_CNT + b * 256 + tid * 16) = (u32x4){0, 0, 0, 0};
    if (cb >= 15) {
        {
            const int task = 2880 + ((i - 24) * 2 + (xcd - 6)) * 8 + wid;
            if (task < NPREP) { PrepRegs p; prep_gather(task, lane, wo, w2, p); WAITV(0); PREP_LANDED(p); prep_store(p, ws); }
            const int sw = (i - 24) * 2 + (xcd - 6);
            const unsigned co = (unsigned)(wid * 80 * 3456 + sw * 216 + min(lane, 53) * 4);
            float lo = 0.f, hi_ = 0.f;
#pragma unroll
            for (int h2 = 0; h2 < 2; ++h2) {
                unsigned v[40];
#pragma unroll
                for (int k = 0; k < 40; ++k) v[k] = *(const unsigned*)(w1 + co + (h2 * 40 + k) * 3456);
#pragma unroll
                for (int k = 0; k < 40; ++k) { lo += bflo(v[k]); hi_ += bfhi(v[k]); }
            }
            LAS f32x2* part = (LAS f32x2*)smem;
            part[wid * 64 + lane] = (f32x2){lo, hi_};
            WAITL0(); BAR();
            if (tid < 54) {
                f32x2 s = part[tid];
#pragma unroll
                for (int k = 1; k < 8; ++k) s += part[k * 64 + tid];
                *(f32x2*)(ws + WS_CS + (sw * 108 + 2 * tid) * 4) = s;
            }
        }
        return;
    }
    LAS char* lds = (LAS char*)smem;
    const char* xb = xin + (size_t)rb * 128 * 1280;
    if (wid >= 4) {
        const int rank = i < 24 ? b : b - 2 * (i - 24);
        g_loader(ws, xb, win, wid - 4, lane, cb * 256, wo, w2, (rank * 4 + wid - 4) * 3, cb >= 5 && cb < 10);
        return;
    }

    const int ct = wid & 1, cc = wid >> 1, r = lane & 31, hi = lane >> 5;
    LAS f32x2* stat = (LAS f32x2*)(lds + G_STAT);
    LAS float* csum = (LAS float*)(lds + G_CS);
    LAS float* n2buf = (LAS float*)lds;
    f32x16 a0 = {}, a1 = {}, a2 = {}, a3 = {};
    f32x4 cs4 = {}, sx4 = {};
    float sq0 = 0.f, sq1 = 0.f;
    const bool vori = cb >= 10;
    {
        const int sw = (r >> 1) & 7;
        u32x4 xa0, xa1; u32x2 wa;
#pragma unroll
        for (int ks = 0; ks < 4; ++ks) { xa0[ks] = (unsigned)((2 * ct + cc) * 4096 + r * 128 + (((2 * ks) ^ hi ^ sw) * 16)); xa1[ks] = xa0[ks] ^ 4096u; }
        const int gam = lane >> 4, q = (lane & 15) >> 2, pp = lane & 3;
#pragma unroll
        for (int v = 0; v < 2; ++v) {
            const int T = 2 * cc + (v ^ ct);
            wa[v] = (unsigned)(16384 + (8 * (gam >> 1) + q) * 256 + (((T ^ q) * 4 + 2 * (gam & 1) + (pp >> 1)) * 16) + (pp & 1) * 8);
        }
        const unsigned selv = ((lane & 15) == (gam & 1)) ? 0x3f803f80u : 0u;
        const u32x4 sel = {selv, selv, selv, selv};
        unsigned cnt;
#define G_OPS : "=&s"(cnt), "+{v[0:15]}"(a0), "+{v[16:31]}"(a1), "+{v[32:47]}"(a2), "+{v[48:63]}"(a3), "+{v[96:99]}"(cs4), "+{v[100:103]}"(sx4), \
                "+{v104}"(sq0), "+{v105}"(sq1), "+{v[112:115]}"(xa0), "+{v[116:119]}"(xa1), "+{v[120:121]}"(wa) \
              : "{v[108:111]}"(sel) \
              : "v64","v65","v66","v67","v68","v69","v70","v71","v72","v73","v74","v75","v76","v77","v78","v79","v80","v81","v82","v83","v84","v85","v86","v87", \
                "v88","v89","v90","v91","v92","v93","v94","v95","v106","v107","scc","memory"
        if (vori) asm volatile(G_LOOP_V G_OPS);
        else asm volatile(G_LOOP_QK G_OPS);
    }
    if (lane < 16) { csum[(2 * cc + ct) * 32 + lane] = cs4[0]; csum[(2 * cc + ct) * 32 + 16 + lane] = cs4[1]; }
    {
        const float sxx = swap32_add(sq0 + sq1);
        const float sx = __uint_as_float(__builtin_amdgcn_permlane16_swap(__float_as_uint(sx4[0]), __float_as_uint(sx4[1]), false, false)[0]);
        const float mu = sx * (1.f / 640.f), var = fmaxf(sxx * (1.f / 640.f) - mu * mu, 0.f);
        if (hi == 0) stat[(2 * ct + cc) * 32 + r] = (f32x2){mu, __builtin_amdgcn_rsqf(var + LN_EPS)};
    }
    WAITL0(); BAR();
#define ACC(u, v) ((u) == 0 ? ((v) == 0 ? a0 : a1) : ((v) == 0 ? a2 : a3))

    if (vori) {
        unsigned vo[2]; float c[2];
#pragma unroll
        for (int v = 0; v < 2; ++v) {
            const int T = 2 * cc + (v ^ ct);
            const int col = (cb - 10) * 128 + T * 32 + r, head = (col * 3277) >> 16, d = col - head * 20;
            vo[v] = (unsigned)((head >> 1) * 10240 + 5120 + (head & 1) * 2560 + hi * 320 + d * 16);
            c[v] = csum[T * 32 + r];
        }
#pragma unroll
        for (int u = 0; u < 2; ++u) {
            const int tt = 2 * ct + (u ^ cc);
            f32x2 st[16];
#pragma unroll
            for (int a = 0; a < 4; ++a) {
                const f32x4 s0 = *(LAS const f32x4*)(stat + tt * 32 + 8 * a + 4 * hi), s1 = *(LAS const f32x4*)(stat + tt * 32 + 8 * a + 4 * hi + 2);
                st[4 * a] = (f32x2){s0[0], s0[1]}; st[4 * a + 1] = (f32x2){s0[2], s0[3]};
                st[4 * a + 2] = (f32x2){s1[0], s1[1]}; st[4 * a + 3] = (f32x2){s1[2], s1[3]};
            }
            const unsigned ob = __builtin_amdgcn_readfirstlane(WS_KV + (unsigned)((rb * 2 + (tt >> 1)) * 163840 + (tt & 1) * 1280));
#pragma unroll
            for (int v = 0; v < 2; ++v) {
                float h[16];
#pragma unroll
                for (int k = 0; k < 16; ++k) h[k] = fmaf(-st[k][0], c[v], ACC(u, v)[k]) * st[k][1];
#pragma unroll
                for (int s = 0; s < 2; ++s) {
                    const u32x4 w = {cvtpk(h[8 * s], h[8 * s + 1]), cvtpk(h[8 * s + 2], h[8 * s + 3]), cvtpk(h[8 * s + 4], h[8 * s + 5]), cvtpk(h[8 * s + 6], h[8 * s + 7])};
                    k1_st128(w, ws, vo[v] + s * 640, ob);
                }
            }
        }
        return;
    }
    const bool isK = cb >= 5;
    const int cbl = isK ? cb - 5 : cb;
    const int head0 = (32 * cbl) / 5;
#pragma unroll
    for (int v = 0; v < 2; ++v) {
        const int T = 2 * cc + (v ^ ct);
        f32x4 c4[4]; unsigned lo[2];
#pragma unroll
        for (int a = 0; a < 4; ++a) c4[a] = *(LAS const f32x4*)(csum + T * 32 + 8 * a + 4 * hi);
#pragma unroll
        for (int a2 = 0; a2 < 2; ++a2) {
            const int gp = 16 * cbl + 4 * T + 2 * a2 + hi, pr = (gp * 205) >> 10;
            lo[a2] = (unsigned)((isK ? pr * 10240 + (gp - 5 * pr) * 1024 : gp * 1024) + r * 16);
        }
#pragma unroll
        for (int u = 0; u < 2; ++u) {
            const int tt = 2 * ct + (u ^ cc);
            const f32x2 st = stat[tt * 32 + r];
            const float sc = isK ? st[1] : st[1] * QSCALE;
            const unsigned ob = __builtin_amdgcn_readfirstlane((isK ? WS_KV + (unsigned)((rb * 2 + (tt >> 1)) * 163840) : WS_Q + (unsigned)((rb * 2 + (tt >> 1)) * 81920)) + (tt & 1) * 512);
            const f32x2 nmu = {-st[0], -st[0]}, sc2 = {sc, sc};
            const f32x16& A = ACC(u, v);
            u32x2 pk[4];
#pragma unroll
            for (int a = 0; a < 4; ++a) {
                const f32x2 h01 = __builtin_elementwise_fma(nmu, (f32x2){c4[a][0], c4[a][1]}, (f32x2){A[4 * a], A[4 * a + 1]}) * sc2;
                const f32x2 h23 = __builtin_elementwise_fma(nmu, (f32x2){c4[a][2], c4[a][3]}, (f32x2){A[4 * a + 2], A[4 * a + 3]}) * sc2;
                pk[a] = (u32x2){cvtpk(h01[0], h01[1]), cvtpk(h23[0], h23[1])};
                if (isK) { const f32x2 q = __builtin_elementwise_fma(h01, h01, h23 * h23); n2buf[(8 * T + 2 * a + hi) * 128 + tt * 32 + r] = q[0] + q[1]; }
            }
#pragma unroll
            for (int a2 = 0; a2 < 2; ++a2) {
                const auto s0 = __builtin_amdgcn_permlane32_swap(pk[2 * a2][0], pk[2 * a2 + 1][0], false, false);
                const auto s1 = __builtin_amdgcn_permlane32_swap(pk[2 * a2][1], pk[2 * a2 + 1][1], false, false);
                k1_st128((u32x4){s0[0], s1[0], s0[1], s1[1]}, ws, lo[a2], ob);
            }
        }
    }
    if (isK) {
        WAITL0(); BAR();
        float m[2];
#pragma unroll
        for (int hl = 0; hl < 2; ++hl) {
            const int lh = wid * 2 + hl;
            const int g0 = max(5 * (head0 + lh) - 32 * cbl, 0), g1 = min(5 * (head0 + lh) + 5 - 32 * cbl, 32);
            float a0[5], a1[5];
#pragma unroll
            for (int k = 0; k < 5; ++k) { const int g = min(g0 + k, 31); a0[k] = n2buf[g * 128 + lane]; a1[k] = n2buf[g * 128 + 64 + lane]; }
            float m0 = 0.f, m1 = 0.f;
#pragma unroll
            for (int k = 0; k < 5; ++k) { const float w_ = g0 + k < g1 ? 1.f : 0.f; m0 = fmaf(a0[k], w_, m0); m1 = fmaf(a1[k], w_, m1); }
            m[hl] = fmaxf(m0, m1);
        }
#pragma unroll
        for (int hl = 0; hl < 2; ++hl) {
            const int head = head0 + wid * 2 + hl, g0 = 5 * head - 32 * cbl;
            const unsigned mx = wave_max63(__float_as_uint(m[hl]));
            if (lane == 63 && head < 32 && g0 < 32) ((unsigned*)(ws + WS_KN))[(head * 2 + (g0 < 0 ? 1 : 0)) * 16 + rb] = mx;
        }
    }
#undef ACC
}

#define FFN1_CLOB "v64", "v65", "v66", "v67", "v68", "v69", "v70", "v71", "v72", "v73", "v74", "v75", "v76", "v77", "v78", "v79", "v80", "v81", "v82", "v83", "v84", "v85", "v86", "v87", "v88", "v89", "v90", "v91", "v92", "v93", "v94", "v95", "v113", "v114", "v115", "v116", "v117", "v118", "v119", "v120", "v121", "v122", "v123", "v124", "v125", "v126", "v127", "v128", "v129", "v130", "v131", "v132", "v133", "v134", "v135", "v136", "v137", "v138", "v139", "v140", "v141", "v142", "v143", "v144", "v145", "v146", "v147", "v148", "v149", "v150", "v151", "v152", "v153", "v154", "v155", "v156", "v157", "v158", "v159", "v160", "v161", "v162", "v163"
constexpr int A_STAGE = 40960, A_RING = 3 * A_STAGE;
constexpr int A_ONES = A_RING;
constexpr int A_FLAG = A_ONES + 33024;
constexpr int D_STAT = 161280 - 1024;
constexpr int D_CS = D_STAT + 1024;
constexpr int D_BIAS = D_CS + 512;
constexpr int D_FLAG = D_BIAS + 512;
constexpr int B_LDS = D_FLAG + 16;
DEV void publish(char* ws, int idx, bool count, bool stored = true) {
    if (stored) WAITV(0);
    WAITL0(); BAR();
    if (count && threadIdx.x == 0) __hip_atomic_fetch_add((GAS unsigned*)(ws + WS_CNT + idx * 256), 1u, __ATOMIC_RELAXED, __HIP_MEMORY_SCOPE_AGENT);
}
DEV void poll(char* ws, int idx, unsigned want) {
    {
        GAS unsigned* c = (GAS unsigned*)(ws + WS_CNT + idx * 256);
        GAS unsigned* tmo = (GAS unsigned*)(ws + WS_CNT + 48 * 256);
        for (unsigned spins = 0;; ++spins) {
            if ((unsigned)__builtin_amdgcn_readfirstlane(__hip_atomic_load(c, __ATOMIC_RELAXED, __HIP_MEMORY_SCOPE_AGENT)) >= want) break;
            if (spins > 100000u) { if ((threadIdx.x & 63) == 0) __hip_atomic_store(tmo, 1u, __ATOMIC_RELAXED, __HIP_MEMORY_SCOPE_AGENT); break; }
            if ((spins & 255u) == 255u && __builtin_amdgcn_readfirstlane(__hip_atomic_load(tmo, __ATOMIC_RELAXED, __HIP_MEMORY_SCOPE_AGENT))) break;
            __builtin_amdgcn_s_sleep(1);
        }
    }
    asm volatile("" ::: "memory");
}
DEV void poll4(char* ws, LAS char* lds, int ldsword, int idx, unsigned want, int wid, unsigned tag) {
    volatile LAS unsigned* f = (volatile LAS unsigned*)(lds + ldsword);
    if (wid == 0) { poll(ws, idx, want); *f = tag; }
    else { for (unsigned spins = 0; *f != tag && spins < 4000000u; ++spins) __builtin_amdgcn_s_sleep(1); }
    asm volatile("" ::: "memory");
}
DEV void await(char* ws, int idx, unsigned want, int wid) {
    if (wid == 0) poll(ws, idx, want);
    BAR();
}

__global__ __launch_bounds__(512, 2) void k_fused(const char* __restrict__ xin, const char* __restrict__ w1, const char* __restrict__ b1,
                                                  const char* __restrict__ b2, char* __restrict__ ws, char* __restrict__ out) {
    extern __shared__ __attribute__((aligned(16))) char smem[];
    const int b = blockIdx.x, xcd = b & 7, i = b >> 3;
    const int tid = threadIdx.x, lane = tid & 63, wid = __builtin_amdgcn_readfirstlane(tid >> 6);
    LAS char* lds = (LAS char*)smem;
    const rsrc_t wsr = __builtin_amdgcn_make_buffer_rsrc(ws, 0, WS_END, 0x00020000);
    const int p = 2 * xcd + (i >> 4), rb = i & 15;
    const int rg = 4 * (xcd >> 1) + (i >> 3), j = 8 * (xcd & 1) + (i & 7);
    const int rb64 = 4 * xcd + (i >> 3), cb = i & 7, rgc = rb64 >> 1;
    const int hp = wid >> 2, head = 2 * p + hp, qt = rb * 4 + (wid & 3);
    const int r = lane & 31, hi = lane >> 5;
    const int tk = lane & 15, g = lane >> 4;
    const unsigned l16 = lane * 16;

    const int kq = wid & 3, mh = wid >> 2;
    u32x2 xres[3];
    const int ctok = rb64 * 64 + (2 * mh + (kq & 1)) * 16 + tk;
#define C_CT(v) (((v) + 3 * (kq >> 1)) % 5)

    {

    const unsigned kbase = WS_KV + (unsigned)(((wid & 3) * 16 + p) * 10240 + hp * 5120);
    const unsigned dsto = (wid & 3) * 10240 + hp * 5120;
    auto issue = [&](int s, int buf, int parts) {
        const unsigned ko = kbase + (unsigned)s * (4 * 163840) + lane * 16;
        const unsigned d = dsto + buf * A_STAGE;
        if (parts & 1) { glds16(ko, ws, d); glds16(ko + 1024, ws, d + 1024); }
        if (parts & 2) { glds16(ko + 2048, ws, d + 2048); glds16(ko + 3072, ws, d + 3072); glds16(ko + 4096, ws, d + 4096); }
    };
    u32x4 q1; u32x2 q2;
    typedef unsigned u32x16 __attribute__((ext_vector_type(16)));
    u32x16 kna, knb;
    {
        const char* knp = ws + WS_KN + head * 128;
        asm volatile("s_load_dwordx16 %0, %2, 0x0\n\ts_load_dwordx16 %1, %2, 0x40" : "=&s"(kna), "=&s"(knb) : "s"(knp) : "memory");
        const unsigned qo = WS_Q + (unsigned)(((qt >> 1) * 80 + 5 * p) * 1024 + (qt & 1) * 512 + r * 16);
        const unsigned o1 = qo + (hp * 3 + hi) * 1024, o2 = qo + 2048 + hp * 8;
        asm volatile("global_load_dwordx4 %0, %2, %4\n\tglobal_load_dwordx2 %1, %3, %4" : "=&v"(q1), "=&v"(q2) : "v"(o1), "v"(o2), "s"(ws) : "memory");
    }
    issue(0, 0, 3); issue(1, 1, 1);
    if (tid < 16) *(LAS u32x4*)(lds + A_ONES + (tid >> 2) * 10240 + (tid & 3) * 640 + 128) = (u32x4){0x3f803f80u, 0x3f803f80u, 0x3f803f80u, 0x3f803f80u};
    asm volatile("s_waitcnt lgkmcnt(0)" : "+s"(kna), "+s"(knb) : : "memory");
    unsigned k2a_ = kna[0], k2b_ = knb[0];
#pragma unroll
    for (int k = 1; k < 16; ++k) { k2a_ = max(k2a_, kna[k]); k2b_ = max(k2b_, knb[k]); }
    const float kn2 = __uint_as_float(k2a_) + ((5 * head) / 32 != (5 * head + 4) / 32 ? __uint_as_float(k2b_) : 0.f);
    asm volatile("s_waitcnt vmcnt(2)" : "+v"(q1), "+v"(q2) : : "memory");
    if (hi) q2 = (u32x2){0, 0};
    float qn2 = sumsq2(q1[0], sumsq2(q1[1], sumsq2(q1[2], sumsq2(q1[3], sumsq2(q2[0], sumsq2(q2[1], 0.f))))));
    qn2 = swap32_add(qn2);
    const float mq = __builtin_amdgcn_sqrtf(qn2 * kn2) * 1.02f + 1e-3f;
    const bool unsafe = !(mq <= 50.f);
    if (tid == 0) *(LAS unsigned*)(lds + D_FLAG) = 0;
    if (lane == 0) *(LAS unsigned*)(lds + A_FLAG + wid * 4) = 0;
    if (__any(unsafe) && lane == 0) *(LAS unsigned*)(lds + A_FLAG + wid * 4) = 1;
    WAITL0(); BAR();
    const u32x4 f0 = lds128(lds + A_FLAG), f1 = lds128(lds + A_FLAG + 16);
    const bool slow = __builtin_amdgcn_readfirstlane(f0[0] | f0[1] | f0[2] | f0[3] | f1[0] | f1[1] | f1[2] | f1[3]) != 0;

    const unsigned ka = (hp * 3 + hi) * 1024 + r * 16;
    const unsigned k2a = 2048 + r * 16 + hp * 8;
    unsigned va = 5120 + hp * 2560 + hi * 320 + r * 16;
    const bool vdata = r < 20 || r >= 28;
    if (r >= 28) va = 5120 + hp * 2560 + hi * 320 + 16 * 16;
    if (!vdata) va = A_ONES + 128;
    f32x16 o = f32x16{};

    if (!slow) {
        f32x16 negm;
#pragma unroll
        for (int k = 0; k < 16; ++k) negm[k] = -mq;
        unsigned kp0 = ka, k2p = k2a, vp = va, vd = 0;
        const unsigned vm = vdata ? 0xffffffffu : 0u;
        int slot = dsto + A_STAGE, d0 = A_STAGE, d1 = A_STAGE, d2 = -2 * A_STAGE;
        unsigned ko = kbase + (unsigned)(4 * 163840) + lane * 16 + 2048;
        u32x4 k1f, v0f, v1f = {0, 0, 0, 0}, p0 = {0, 0, 0, 0}, p1 = {0, 0, 0, 0}; u32x2 k2f;
        f32x4 t0 = {}, t1 = {};
        f32x16 sa, sb = {};
        {
            sa = mfma32(lds128(lds + ka), q1, negm); sa = mfma32k8(lds64(lds + k2a), q2, sa);
            k1f = lds128(lds + ka + 512); k2f = lds64(lds + k2a + 512);
            v0f = lds128(lds + va);
        }
#define A_OPS : "+{v[0:15]}"(o), "+{v[16:31]}"(sa), "+{v[32:47]}"(sb), "+{v[70:73]}"(k1f), "+{v[74:75]}"(k2f), "+{v[76:79]}"(v0f), "+{v[80:83]}"(v1f), \
                "+{v[84:87]}"(p0), "+{v[88:91]}"(p1), "+{v[92:95]}"(t0), "+{v[96:99]}"(t1), "+{v100}"(kp0), "+{v102}"(k2p), "+{v103}"(vp), \
                "+{v104}"(vd), "+{v105}"(ko), "+{v108}"(tdummy), "+{v109}"(tw1), "+{v110}"(two), [n] "+s"(n), [slot] "+s"(slot), [d0] "+s"(d0), [d1] "+s"(d1), [d2] "+s"(d2), [t0] "=&s"(st0) \
              : "{v[48:63]}"(negm), "{v[64:67]}"(q1), "{v[68:69]}"(q2), "{v107}"(vm), [ws] "s"(ws), [w1r] "s"(w1r), [wor] "s"(wor) : "scc", "memory"
        int n = 6, st0;
        const rsrc_t w1r = __builtin_amdgcn_make_buffer_rsrc((void*)w1, 0, 640 * 3456, 0x00020000);
        const rsrc_t wor = __builtin_amdgcn_make_buffer_rsrc(ws + WS_WO, 0, 40 * 20 * 1024 + 40 * 28 * 1024, 0x00020000);
        const int tl8 = (i * 8 + wid) * 10 + lane;
        unsigned tw1 = (unsigned)((tl8 >> 4) * 3456 + (xcd & 1) * 2048 + (tl8 & 15) * 128), two = (unsigned)(tl8 * 128), tdummy = 0;
        asm volatile(ATTN_LOOP A_OPS);
        p1[2] = cvtpk(t1[0], t1[1]); p1[3] = cvtpk(t1[2], t1[3]);
        o = mfma32(v1f, p1, o);
    } else {
        float m = -INFINITY;
        int buf = 0;
        issue(1, 1, 2); issue(2, 2, 3);
#pragma unroll 1
        for (int s = 0; s < 8; ++s) {
            if (s > 0) {
                WAITV(0); WAITL0(); BAR();
                if (s >= 1 && s < 6) issue(s + 2, buf == 0 ? 2 : buf - 1, 3);
            }
            const unsigned so = buf * A_STAGE, vo = vdata ? so : 0;
#pragma unroll 1
            for (int t = 0; t < 8; ++t) {
                const unsigned to = so + (t >> 1) * 10240 + (t & 1) * 512;
                const u32x4 k1f = lds128(lds + ka + to); const u32x2 k2f = lds64(lds + k2a + to);
                const unsigned vt = va + vo + (t >> 1) * 10240 + (t & 1) * 1280;
                const u32x4 v0f = lds128(lds + vt), v1f = lds128(lds + vt + 640);
                f32x16 sc = mfma32(k1f, q1, f32x16{}); sc = mfma32k8(k2f, q2, sc);
                float tm = sc[0];
#pragma unroll
                for (int k = 1; k < 16; ++k) tm = fmaxf(tm, sc[k]);
                tm = swap32_max(tm);
                const float mn = fmaxf(m, tm), al = __builtin_amdgcn_exp2f(m - mn);
                m = mn;
#pragma unroll
                for (int k = 0; k < 16; ++k) { o[k] *= al; sc[k] = __builtin_amdgcn_exp2f(sc[k] - mn); }
                const u32x4 p0 = {cvtpk(sc[0], sc[1]), cvtpk(sc[2], sc[3]), cvtpk(sc[4], sc[5]), cvtpk(sc[6], sc[7])};
                const u32x4 p1 = {cvtpk(sc[8], sc[9]), cvtpk(sc[10], sc[11]), cvtpk(sc[12], sc[13]), cvtpk(sc[14], sc[15])};
                o = mfma32(v0f, p0, o); o = mfma32(v1f, p1, o);
            }
            buf = buf == 2 ? 0 : buf + 1;
        }
    }
    const float lo_ = other32(o[8], hi);
    const float l = hi ? o[8] : lo_;
    const float il = __builtin_amdgcn_rcpf(l);
    const int tok = qt * 32 + r;
    const unsigned oo = WS_O + (unsigned)((((tok >> 4) * 80 + 5 * p) * 16 + (tok & 15)) * 16);
    st128_dev((u32x4){cvtpk(o[0] * il, o[1] * il), cvtpk(o[2] * il, o[3] * il), cvtpk(o[4] * il, o[5] * il), cvtpk(o[6] * il, o[7] * il)}, wsr, oo + (2 * hp + hi) * 256, 0);
    if (hi == 0) st64_dev((u32x2){cvtpk(o[8] * il, o[9] * il), cvtpk(o[10] * il, o[11] * il)}, wsr, oo + 4 * 256 + hp * 8, 0);
    }
    publish(ws, rb, true);
    {
        const char* wsrc = ws + WS_WO + (unsigned)(cb * 100 * 1024);
#pragma unroll
        for (int k = 0; k < 13; ++k) { const int f = wid + 8 * k; if (f < 100) glds16n(l16, wsrc + f * 1024, f * 1024); }
#pragma unroll
        for (int v = 0; v < 3; ++v) xres[v] = *(const u32x2*)(xin + (unsigned)(ctok * 1280 + (cb * 80 + C_CT(v) * 16 + 4 * g) * 2));
    }

    const int dl = wid - 4;
    unsigned dwo = 0;
    {
        const int r4 = lane >> 4, p16 = lane & 15;
        int cbyte = j * 256 + ((p16 ^ (r4 << 2)) * 16);
        if (cbyte >= 3456) cbyte -= 128;
        dwo = (unsigned)(r4 * 3456 + cbyte);
    }
    auto d_issue = [&](int s) {
        const char* wp = w1 + (size_t)(s * 64 + dl * 16) * 3456;
        const unsigned dst = (unsigned)(s % 6) * 16384 + (unsigned)dl * 4096;
#pragma unroll
        for (int k = 0; k < 4; ++k) { glds16n(dwo, wp + (size_t)k * 4 * 3456, dst + k * 1024); }
    };

    await(ws, rgc, 16, wid);
    {
        f32x4 acc[2][5];
#pragma unroll
        for (int u = 0; u < 2; ++u)
#pragma unroll
            for (int v = 0; v < 5; ++v) acc[u][v] = f32x4{};
        u32x4 af[5][2];
#pragma unroll
        for (int n = 0; n < 5; ++n)
#pragma unroll
            for (int u = 0; u < 2; ++u) af[n][u] = ld128_dev(wsr, l16, WS_O + (unsigned)(((rb64 * 4 + 2 * mh + (u ^ (kq & 1))) * 80 + 4 * (kq + 4 * n)) * 256));
        SBAR();
        WAITV(10); BAR();
#pragma unroll
        for (int n = 0; n < 5; ++n) {
            u32x4 wf[5];
#pragma unroll
            for (int v = 0; v < 5; ++v) wf[v] = lds128(lds + (C_CT(v) * 20 + kq + 4 * n) * 1024 + l16);
#pragma unroll
            for (int u = 0; u < 2; ++u)
#pragma unroll
                for (int v = 0; v < 5; ++v) acc[u][v] = mfma16(wf[v], af[n][u], acc[u][v]);
        }
        WAITL0(); BAR();
        auto slot_of = [&](int ta, int ct) { const int owner = ta + (ct >= 3 ? 2 : 0); return ((((mh * 2 + ta) * 5 + ct) * 3 + ((kq - owner + 4) & 3) - 1) << 10) + l16; };
#pragma unroll
        for (int v = 0; v < 5; ++v) *(LAS f32x4*)(lds + slot_of((kq & 1) ^ 1, C_CT(v))) = acc[1][v];
        *(LAS f32x4*)(lds + slot_of(kq & 1, C_CT(3))) = acc[0][3];
        *(LAS f32x4*)(lds + slot_of(kq & 1, C_CT(4))) = acc[0][4];
        if (kq >= 2) *(LAS f32x4*)(lds + slot_of(kq & 1, C_CT(2))) = acc[0][2];
        WAITL0(); BAR();
        float s1 = 0.f, s2 = 0.f;
#pragma unroll
        for (int v = 0; v < 3; ++v) {
            if (v < 2 || kq < 2) {
                const int ct = C_CT(v);
                f32x4 a = acc[0][v];
                const unsigned rs = ((((mh * 2 + (kq & 1)) * 5 + ct) * 3) << 10) + l16;
#pragma unroll
                for (int k = 0; k < 3; ++k) a += *(LAS const f32x4*)(lds + rs + k * 1024);
                a += (f32x4){bflo(xres[v][0]), bfhi(xres[v][0]), bflo(xres[v][1]), bfhi(xres[v][1])};
                const u32x2 o2 = {cvtpk(a[0], a[1]), cvtpk(a[2], a[3])};
                st64_dev(o2, wsr, WS_H + (unsigned)((((ctok >> 5) * 40 + 5 * cb + ct) * 64 + (g >> 1) * 32 + (ctok & 31)) * 16 + (g & 1) * 8), 0);
                const float h0 = bflo(o2[0]), h1 = bfhi(o2[0]), h2 = bflo(o2[1]), h3 = bfhi(o2[1]);
                s1 += (h0 + h1) + (h2 + h3); s2 += fmaf(h0, h0, h1 * h1) + fmaf(h2, h2, h3 * h3);
            }
        }
        s1 += __shfl_xor(s1, 16); s2 += __shfl_xor(s2, 16);
        s1 = swap32_add(s1); s2 = swap32_add(s2);
        if (g == 0) st64_dev((u32x2){__float_as_uint(s1), __float_as_uint(s2)}, wsr, WS_ST + (unsigned)((((ctok >> 7) * 16 + cb * 2 + (kq >> 1)) * 128 + (ctok & 127)) * 8), 0);
    }
    publish(ws, 16 + rgc, true);
    if (wid >= 4 && j < 14) { d_issue(0); d_issue(1); d_issue(2); }

    constexpr int NK = 54;
    const int ew = wid & 3, e_cnt = (NK - ew + 3) / 4;
#define E_SLOT(w_, n_) ((n_) < 3 ? 98304 + ((w_) * 3 + (n_)) * 5120 : ((w_) * 4 + (n_) - 3) * 5120)
    auto e_stage = [&](int n) {
        const char* src = ws + WS_W2 + (unsigned)((cb * 5 * 28 + ew + 4 * n) * 1024);
#pragma unroll
        for (int v = 0; v < 5; ++v) glds16n(l16, src + v * 28 * 1024, (unsigned)(E_SLOT(ew, n) + v * 1024));
    };

    if (j < 14) {
        LAS f32x2* stat = (LAS f32x2*)(lds + D_STAT);
        LAS float* csum = (LAS float*)(lds + D_CS);
        LAS float* bias = (LAS float*)(lds + D_BIAS);
        if (wid >= 4) {
            WAITV(8); BAR(); d_issue(3);
            WAITV(8); BAR(); d_issue(4);
            WAITV(8); BAR(); d_issue(5);
            WAITV(8); BAR(); d_issue(6);
            WAITV(8); BAR(); d_issue(7);
            WAITV(8); BAR(); d_issue(8);
            WAITV(8); BAR(); d_issue(9);
            WAITV(8); BAR();
            e_stage(0);
            u32x4 sv[16]; unsigned bv = 0; u32x2 cv = {0, 0};
            const int bc = j * 128 + 2 * lane;
            if (wid == 4) {
#pragma unroll
                for (int k = 0; k < 16; ++k)
                    asm volatile("buffer_load_dwordx4 %0, %1, %2, %3 offen sc1" : "=&v"(sv[k]) : "v"(l16), "s"(wsr), "s"(WS_ST + (unsigned)((rg * 16 + k) * 1024)) : "memory");
            } else if (wid == 5) {
                asm volatile("global_load_dword %0, %2, %4\n\tglobal_load_dwordx2 %1, %3, %5" : "=&v"(bv), "=&v"(cv)
                             : "v"((unsigned)((bc < 1728 ? bc : 0) * 2)), "v"(WS_CS + (unsigned)((bc < 1728 ? bc : 0) * 4)), "s"(b1), "s"(ws) : "memory");
            }
            if (wid == 4) WAITV(25); else WAITV(9);
            BAR();
            e_stage(1);
            if (wid == 4) WAITV(26); else if (wid == 5) WAITV(12); else WAITV(10);
            BAR();
            WAITV(5);
            if (wid == 4) {
                asm volatile("" : "+v"(sv[0]), "+v"(sv[1]), "+v"(sv[2]), "+v"(sv[3]), "+v"(sv[4]), "+v"(sv[5]), "+v"(sv[6]), "+v"(sv[7]));
                asm volatile("" : "+v"(sv[8]), "+v"(sv[9]), "+v"(sv[10]), "+v"(sv[11]), "+v"(sv[12]), "+v"(sv[13]), "+v"(sv[14]), "+v"(sv[15]));
                f32x4 s = {};
#pragma unroll
                for (int k = 0; k < 16; ++k) s += __builtin_bit_cast(f32x4, sv[k]);
                const float m0 = s[0] * (1.f / 640.f), v0 = fmaxf(s[1] * (1.f / 640.f) - m0 * m0, 0.f);
                const float m1 = s[2] * (1.f / 640.f), v1 = fmaxf(s[3] * (1.f / 640.f) - m1 * m1, 0.f);
                *(LAS f32x4*)(stat + 2 * lane) = (f32x4){m0, __builtin_amdgcn_rsqf(v0 + LN_EPS), m1, __builtin_amdgcn_rsqf(v1 + LN_EPS)};
            } else if (wid == 5) {
                asm volatile("" : "+v"(bv), "+v"(cv));
                *(LAS f32x2*)(bias + 2 * lane) = bc < 1728 ? (f32x2){bflo(bv), bfhi(bv)} : (f32x2){0.f, 0.f};
                *(LAS u32x2*)(csum + 2 * lane) = cv;
            }
            e_stage(2);
            WAITL0();
            BAR();
        } else {
            poll4(ws, lds, D_FLAG, 16 + rg, 16, wid, 1);
            f32x16 a0 = {}, a1 = {}, a2 = {}, a3 = {};
            {
                u32x4 waA, waB;
                const int gam = lane >> 4, q = (lane & 15) >> 2, pp = lane & 3;
#pragma unroll
                for (int v = 0; v < 4; ++v) {
                    const int T = (v + wid) & 3;
                    waA[v] = (unsigned)((8 * (gam >> 1) + q) * 256 + (((T ^ q) * 4 + 2 * (gam & 1) + (pp >> 1)) * 16) + (pp & 1) * 8);
                    waB[v] = waA[v] + 49152;
                }
                unsigned soff = WS_H + (unsigned)((rg * 4 + wid) * 40 * 1024);
                asm volatile(FFN1_LOOP
                    : "+{v[0:15]}"(a0), "+{v[16:31]}"(a1), "+{v[32:47]}"(a2), "+{v[48:63]}"(a3), [soff] "+s"(soff)
                    : "{v[104:107]}"(waA), "{v[108:111]}"(waB), "{v112}"(l16), [srd] "s"(wsr)
                    : FFN1_CLOB, "scc", "memory");
            }
            BAR();
            const f32x2 st = stat[wid * 32 + r];
#pragma unroll
            for (int v = 0; v < 4; ++v) {
                const int T = (v + wid) & 3, cb32 = j * 4 + T;
                if (cb32 < 54) {
                    const f32x16& A = v == 0 ? a0 : v == 1 ? a1 : v == 2 ? a2 : a3;
                    float h[16];
                    const f32x2 nmu = {-st[0], -st[0]}, rs = {st[1], st[1]};
#pragma unroll
                    for (int a = 0; a < 4; ++a) {
                        const f32x4 c4 = *(LAS const f32x4*)(csum + T * 32 + 8 * a + 4 * hi), b4 = *(LAS const f32x4*)(bias + T * 32 + 8 * a + 4 * hi);
#pragma unroll
                        for (int k = 0; k < 4; k += 2) {
                            const f32x2 y = __builtin_elementwise_fma(nmu, (f32x2){c4[k], c4[k + 1]}, (f32x2){A[4 * a + k], A[4 * a + k + 1]});
                            const f32x2 z = __builtin_elementwise_fma(y, rs, (f32x2){b4[k], b4[k + 1]});
                            const f32x2 rl = {fmaxf(z[0], 0.f), fmaxf(z[1], 0.f)}, q = rl * rl;
                            h[4 * a + k] = q[0]; h[4 * a + k + 1] = q[1];
                        }
                    }
                    const unsigned so = WS_HID + (unsigned)(((rg * 8 + wid * 2) * 216 + cb32 * 4) * 256);
                    const unsigned lo = (unsigned)((r >> 4) * 216 * 256 + hi * 256 + (r & 15) * 16);
#pragma unroll
                    for (int s = 0; s < 2; ++s)
                        st128_dev((u32x4){cvtpk(h[8 * s], h[8 * s + 1]), cvtpk(h[8 * s + 2], h[8 * s + 3]), cvtpk(h[8 * s + 4], h[8 * s + 5]), cvtpk(h[8 * s + 6], h[8 * s + 7])}, wsr, lo + s * 512, so);
                }
            }
        }
    }
    else {
        if (wid >= 4) { e_stage(0); e_stage(1); e_stage(2); WAITV(0); }
    }
    publish(ws, 32 + rg, j < 14, wid < 4);
    if (wid >= 4) {
        e_stage(3); e_stage(4); e_stage(5); e_stage(6);
        WAITV(0);
        BAR();
        return;
    }

    {
        constexpr int NMAX = 14;
        const int w = wid;
        unsigned ao[4];
#pragma unroll
        for (int u = 0; u < 4; ++u) ao[u] = WS_HID + (unsigned)((rb64 * 4 + ((u + w) & 3)) * 216 * 256);
        u32x4 af[3][4], bf[3][5];
        const char* wfp = ws + WS_W2 + (unsigned)((40 * 28 + cb * 5 * 26 + w - 28) * 1024) + l16;
#define E_W(slot, n, v) do { if ((n) < 7) bf[slot][v] = lds128(lds + E_SLOT(w, n) + (v) * 1024 + l16); \
                             else bf[slot][v] = *(const u32x4*)(wfp + ((v) * 26 + 4 * (n)) * 1024); } while (0)
#define E_A(slot, u, n) af[slot][u] = ld128_dev(wsr, l16, ao[u] + (unsigned)((w + 4 * (n)) * 1024))
        const int tok = rb64 * 64 + w * 16 + tk;
        u32x2 res[5], bb[5];
#pragma unroll
        for (int v = 0; v < 5; ++v) {
            res[v] = ld64_dev(wsr, WS_H + (unsigned)((((tok >> 5) * 40 + 5 * cb + v) * 64 + (g >> 1) * 32 + (tok & 31)) * 16 + (g & 1) * 8), 0);
            bb[v] = *(const u32x2*)(b2 + (cb * 80 + 4 * g) * 2 + v * 32);
        }
        SBAR();
        poll4(ws, lds, D_FLAG, 32 + rgc, 14, wid, 2);
        f32x4 acc[4][5];
#pragma unroll
        for (int u = 0; u < 4; ++u)
#pragma unroll
            for (int v = 0; v < 5; ++v) acc[u][v] = f32x4{};
#pragma unroll
        for (int n = 0; n < 2; ++n) { E_A(n, 0, n); E_A(n, 1, n); E_A(n, 2, n); E_A(n, 3, n); }
        SBAR();
#pragma unroll
        for (int v = 0; v < 5; ++v) E_W(0, 0, v);
        SBAR();
#pragma unroll
        for (int n = 0; n < NMAX; ++n) {
            if (n < NK / 4 || n < e_cnt) {
                const bool more = n + 2 < NMAX && (n + 2 < NK / 4 || n + 2 < e_cnt);
                const int sl = n % 3, nl = (n + 2) % 3;
#pragma unroll
                for (int u = 0; u < 4; ++u) {
#pragma unroll
                    for (int v = 0; v < 5; ++v) acc[u][v] = mfma16(bf[sl][v], af[sl][u], acc[u][v]);
                    SBAR();
                    if (n == 0 && u == 0) {
                        BAR();
#pragma unroll
                        for (int v = 0; v < 5; ++v) E_W(1, 1, v);
                    }
                    if (more) {
                        if (u == 0) { E_A(nl, 0, n + 2); E_A(nl, 1, n + 2); }
                        if (u == 1) { E_A(nl, 2, n + 2); E_A(nl, 3, n + 2); }
                        if (u == 2) { E_W(nl, n + 2, 0); E_W(nl, n + 2, 1); E_W(nl, n + 2, 2); }
                        if (u == 3) { E_W(nl, n + 2, 3); E_W(nl, n + 2, 4); }
                    }
                    SBAR();
                }
            }
        }
        WAITL0(); BAR();
#pragma unroll
        for (int u = 1; u < 4; ++u)
#pragma unroll
            for (int v = 0; v < 5; ++v) *(LAS f32x4*)(lds + ((((u + w) & 3) * 3 + (u - 1)) * 5 + v) * 1024 + l16) = acc[u][v];
        WAITL0(); BAR();
        const unsigned ro = (unsigned)(tok * 1280 + (cb * 80 + 4 * g) * 2);
#pragma unroll
        for (int v = 0; v < 5; ++v) {
            f32x4 a = acc[0][v];
#pragma unroll
            for (int k = 0; k < 3; ++k) a += *(LAS const f32x4*)(lds + ((w * 3 + k) * 5 + v) * 1024 + l16);
            a += (f32x4){bflo(res[v][0]), bfhi(res[v][0]), bflo(res[v][1]), bfhi(res[v][1])};
            a += (f32x4){bflo(bb[v][0]), bfhi(bb[v][0]), bflo(bb[v][1]), bfhi(bb[v][1])};
            *(u32x2*)(out + ro + v * 32) = (u32x2){cvtpk(a[0], a[1]), cvtpk(a[2], a[3])};
        }
    }
}

extern "C" void kernel_launch(void* const* d_in, const int* in_sizes, int n_in, void* d_out, int out_size, void* d_ws, size_t ws_size,
                              hipStream_t stream) {
    const char* x = (const char*)d_in[0];
    const char* wqkv = (const char*)d_in[1];
    const char* wo = (const char*)d_in[2];
    const char* w1 = (const char*)d_in[4];
    const char* b1 = (const char*)d_in[5];
    const char* w2 = (const char*)d_in[6];
    const char* b2 = (const char*)d_in[7];
    char* ws = (char*)d_ws;
    (void)hipFuncSetAttribute((const void*)k_qkv, hipFuncAttributeMaxDynamicSharedMemorySize, G_LDS);
    (void)hipFuncSetAttribute((const void*)k_fused, hipFuncAttributeMaxDynamicSharedMemorySize, B_LDS);
    k_qkv<<<256, 512, G_LDS, stream>>>(x, wqkv, w1, wo, w2, ws);
    k_fused<<<256, 512, B_LDS, stream>>>(x, w1, b1, b2, ws, (char*)d_out);
}
```
